```python
import torch

def _softmax(x, axis=-1):
    return torch.softmax(x, dim=axis)
def _gelu(x):
    return torch.nn.functional.gelu(x, approximate="tanh")
def _silu(x):
    return torch.nn.functional.silu(x)
def _sigmoid(x):
    return torch.sigmoid(x)
def _layernorm(x, axis=-1, eps=1e-5):
    m = x.mean(dim=axis, keepdim=True)
    v = x.var(dim=axis, keepdim=True, unbiased=False)
    return (x - m) / torch.sqrt(v + eps)
def _scatter_add(tgt, idx, src, axis=0):
    return tgt.clone().index_add_(axis, idx, src)
def _segment_sum(src, seg, n):
    out = torch.zeros((n,) + src.shape[1:], dtype=src.dtype)
    return out.index_add_(0, seg, src)
def _pad(x, pw):
    flat = tuple(v for pair in reversed(pw) for v in pair)
    return torch.nn.functional.pad(x, flat)

kernel_name = 'interleave_layers__spectral_fnet__moe_arch_mod__prompt'

def setup_inputs():

    _w = (lambda a: (lambda t: t.to(torch.bfloat16) if t.is_floating_point() else t)(torch.as_tensor(a)))
    def _A_setup():
        B, S, D = 1, 2048, 640
        ns = torch.arange(S)[:, None] * torch.arange(S)[None, :]
        nd = torch.arange(D)[:, None] * torch.arange(D)[None, :]
        Ws_re = torch.cos(-2.0 * torch.pi * ns / S).to(float)
        Ws_im = torch.sin(-2.0 * torch.pi * ns / S).to(float)
        Wd_re = torch.cos(-2.0 * torch.pi * nd / D).to(float)
        Wd_im = torch.sin(-2.0 * torch.pi * nd / D).to(float)
        return {
            "x": _w((lambda *s: torch.randn(*s, dtype=torch.bfloat16) * 0.1)(B, S, D)),
            "dft_seq_re": _w(Ws_re),
            "dft_seq_im": _w(Ws_im),
            "dft_hid_re": _w(Wd_re),
            "dft_hid_im": _w(Wd_im),
        }
    _A_in = _A_setup()

    def _B_setup():
        B, S, D, F, C = 1, 2048, 640, 1728, 8
        return {
            "x": _w((lambda *s: torch.randn(*s, dtype=torch.bfloat16) * 0.1)(B, S, D)),
            "w_r": _w((lambda *s: torch.randn(*s, dtype=torch.bfloat16) * 1.6)(D)),
            "w_gate": _w((lambda *s: torch.randn(*s, dtype=torch.bfloat16) * 0.4)(D, F)),
            "w_up": _w((lambda *s: torch.randn(*s, dtype=torch.bfloat16) * 0.4)(D, F)),
            "w_down": _w((lambda *s: torch.randn(*s, dtype=torch.bfloat16) * 0.4)(F, D)),
        }
    _B_in = _B_setup()

    return {
        "A_x": _A_in["x"],
        "A_dft_seq_re": _A_in["dft_seq_re"],
        "A_dft_seq_im": _A_in["dft_seq_im"],
        "A_dft_hid_re": _A_in["dft_hid_re"],
        "A_dft_hid_im": _A_in["dft_hid_im"],
        "B_x": _B_in["x"],
        "B_w_r": _B_in["w_r"],
        "B_w_gate": _B_in["w_gate"],
        "B_w_up": _B_in["w_up"],
        "B_w_down": _B_in["w_down"],
    }

def reference(A_x, A_dft_seq_re, A_dft_seq_im, A_dft_hid_re, A_dft_hid_im, B_x, B_w_r, B_w_gate, B_w_up, B_w_down):
    def _A_ref(x, dft_seq_re, dft_seq_im, dft_hid_re, dft_hid_im):
        a = torch.einsum("bsd,ed->bse", x, dft_hid_re)
        b = torch.einsum("bsd,ed->bse", x, dft_hid_im)
        y = torch.einsum("ts,bse->bte", dft_seq_re, a) - torch.einsum("ts,bse->bte", dft_seq_im, b)
        return y

    def _B_ref(x, w_r, w_gate, w_up, w_down):
        B, S, D = x.shape
        C = 8
        r = x @ w_r
        idx = torch.argsort(-r, axis=-1)[:, :C]
        idx_e = (lambda x, n, axis: torch.repeat_interleave(x, n, dim=axis))(idx.reshape(B, C, 1), D, axis=2)
        x_sel = (lambda x, i, axis: torch.take_along_dim(x, i, dim=axis))(x, idx_e, axis=1)
        g = _silu(x_sel @ w_gate)
        u = x_sel @ w_up
        y = (g * u) @ w_down
        r_sel = _sigmoid((lambda x, i, axis: torch.take_along_dim(x, i, dim=axis))(r, idx, axis=1))
        y = y * r_sel.reshape(B, C, 1)
        b_off = (torch.arange(B) * S).reshape(B, 1)
        flat_idx = (idx + b_off).reshape(B * C)
        zero = (x * 0.0).reshape(B * S, D)
        upd = _scatter_add(zero, flat_idx, y.reshape(B * C, D), axis=0)
        return x + upd.reshape(B, S, D)

    x = A_x
    x = x + _A_ref(_layernorm(x, axis=-1), A_dft_seq_re, A_dft_seq_im, A_dft_hid_re, A_dft_hid_im)
    x = x + _B_ref(_layernorm(x, axis=-1), B_w_r, B_w_gate, B_w_up, B_w_down)
    x = x + _A_ref(_layernorm(x, axis=-1), A_dft_seq_re, A_dft_seq_im, A_dft_hid_re, A_dft_hid_im)
    x = x + _B_ref(_layernorm(x, axis=-1), B_w_r, B_w_gate, B_w_up, B_w_down)
    return x
```

```cpp
#include <hip/hip_runtime.h>
#include <hip/hip_bf16.h>
#include <type_traits>

typedef unsigned short u16;
typedef __attribute__((address_space(3))) void* lds_ptr_t;
using bf16x8 = __attribute__((ext_vector_type(8))) __bf16;
using bf16x2 = __attribute__((ext_vector_type(2))) __bf16;
using f32x4 = __attribute__((ext_vector_type(4))) float;
using f32x2 = __attribute__((ext_vector_type(2))) float;
using u32x2 = __attribute__((ext_vector_type(2))) unsigned;
using u32x4 = __attribute__((ext_vector_type(4))) unsigned;

struct Params {
  const u16 *x0, *Cr, *Ci, *Hr, *Hi, *wr, *wg, *wu, *wd;
  u16* out;
  char* ws;
};

constexpr int SZ_AB = 2 * 8 * 64 * 5 * 1024;
constexpr int SZ_X = 2048 * 640 * 2;
constexpr int WS_HO = 0;
constexpr int HO_H2 = 0;
constexpr int HO_H4 = 27648;
constexpr int HO_YN = 55296;
constexpr int HO_YB = 65536;
constexpr int HO_KEY3 = 75776;
constexpr int HO_XS3 = 83968;
constexpr int HO_XR3 = 94208;
constexpr int HO_IDX2 = 104448;
constexpr int HO_KS3 = 104704;
constexpr int HO_BYTES = 131072;
constexpr int WS_T = WS_HO + HO_BYTES;
constexpr int WS_CS = WS_T + 40960;
constexpr int WS_ST = WS_CS + 65536;
constexpr int WS_KEY1 = WS_ST + 32768;
constexpr int WS_X1 = WS_KEY1 + 8192;
constexpr int WS_AB1 = WS_X1 + SZ_X;
constexpr int WS_AB3 = WS_AB1 + SZ_AB;
constexpr int WS_P1 = WS_AB3 + SZ_AB;
constexpr int WS_P3 = WS_P1 + 4 * SZ_X;

constexpr float EPS = 1e-5f;
constexpr unsigned SENT = 0xFFFFFFFFu;
constexpr int SPIN_CAP = 1 << 18;

#if defined(__HIP_DEVICE_COMPILE__)
#define DEV __device__ __forceinline__
typedef __amdgpu_buffer_rsrc_t rsrc_t;
DEV rsrc_t mk(const void* p, int bytes) { return __builtin_amdgcn_make_buffer_rsrc((void*)p, 0, bytes, 0x00020000); }
template <int N> DEV void wait_vm() { asm volatile("s_waitcnt vmcnt(%0)" ::"i"(N) : "memory"); }
DEV void wait_lgkm0() { __builtin_amdgcn_s_waitcnt(0xC07F); }
DEV void bar() { __builtin_amdgcn_s_barrier(); }
DEV void lds_bar() { asm volatile("s_waitcnt lgkmcnt(0)\n\ts_barrier" ::: "memory"); }
DEV unsigned pk2(float a, float b) { f32x2 v = {a, b}; return __builtin_bit_cast(unsigned, __builtin_convertvector(v, bf16x2)); }
DEV float lo(unsigned v) { return __builtin_bit_cast(float, v << 16); }
DEV float hi(unsigned v) { return __builtin_bit_cast(float, v & 0xffff0000u); }
DEV float rb(float a) { return lo(pk2(a, a)); }
DEV float dot2(unsigned a, unsigned b, float c) { return __builtin_amdgcn_fdot2_f32_bf16(__builtin_bit_cast(bf16x2, a), __builtin_bit_cast(bf16x2, b), c, false); }
DEV f32x4 mfma(u32x4 a, u32x4 b, f32x4 c) { return __builtin_amdgcn_mfma_f32_16x16x32_bf16(__builtin_bit_cast(bf16x8, a), __builtin_bit_cast(bf16x8, b), c, 0, 0, 0); }
DEV u32x4 ld16(rsrc_t r, int v, int s) { return __builtin_amdgcn_raw_buffer_load_b128(r, v, s, 0); }
DEV u32x4 ld16c(rsrc_t r, int v, int s) { return __builtin_amdgcn_raw_buffer_load_b128(r, v, s, 16); }
DEV unsigned ld4(rsrc_t r, int v, int s) { return __builtin_amdgcn_raw_buffer_load_b32(r, v, s, 0); }
DEV unsigned ld4c(rsrc_t r, int v, int s) { return __builtin_amdgcn_raw_buffer_load_b32(r, v, s, 16); }
DEV void glds(rsrc_t r, char* l, int v, int s) { __builtin_amdgcn_raw_ptr_buffer_load_lds(r, (lds_ptr_t)l, 16, v, s, 0, 0); }
DEV float xq_sum(float x) {
  auto s = __builtin_amdgcn_permlane16_swap(__builtin_bit_cast(unsigned, x), __builtin_bit_cast(unsigned, x), false, false);
  x = __builtin_bit_cast(float, (unsigned)s[0]) + __builtin_bit_cast(float, (unsigned)s[1]);
  auto t = __builtin_amdgcn_permlane32_swap(__builtin_bit_cast(unsigned, x), __builtin_bit_cast(unsigned, x), false, false);
  return __builtin_bit_cast(float, (unsigned)t[0]) + __builtin_bit_cast(float, (unsigned)t[1]);
}
DEV unsigned nosent(unsigned v) { return min(v, 0xFFFFFFFEu); }
DEV bool has_sent(u32x4 v) { return v[0] == SENT || v[1] == SENT || v[2] == SENT || v[3] == SENT; }
DEV unsigned mkkey(float r, int t) {
  unsigned b = pk2(r, r) & 0xffffu;
  b = (b & 0x8000u) ? (~b & 0xffffu) : (b | 0x8000u);
  return (b << 16) | (unsigned)(2047 - t);
}
DEV int key_row(unsigned k) { return 2047 - (int)(k & 0x7ffu); }
DEV float key_val(unsigned k) { unsigned b = k >> 16; b = (b & 0x8000u) ? (b & 0x7fffu) : (~b & 0xffffu); return __builtin_bit_cast(float, b << 16); }
DEV float sigm(float x) { return __builtin_amdgcn_rcpf(1.f + __expf(-x)); }
template <int C> DEV unsigned dpp_max(unsigned m) { return max(m, (unsigned)__builtin_amdgcn_mov_dpp((int)m, C, 0xf, 0xf, true)); }

template <int NT> DEV void top8(const u32x4& ka, const u32x4& kb, unsigned* sc, int tid) {
  const int lane = tid & 63;
  if (tid < 256) {
    unsigned m = max(max(max(ka[0], ka[1]), max(ka[2], ka[3])), max(max(kb[0], kb[1]), max(kb[2], kb[3])));
    m = dpp_max<0xB1>(m); m = dpp_max<0x4E>(m); m = dpp_max<0x141>(m); m = dpp_max<0x140>(m);
    auto s = __builtin_amdgcn_permlane16_swap(m, m, false, false);
    m = max((unsigned)s[0], (unsigned)s[1]);
    if ((lane & 31) == 0) sc[tid >> 5] = m;
    if (tid == 0) sc[8] = 0;
  }
  lds_bar();
  if (tid < 256) {
    u32x4 g0 = *(const u32x4*)sc, g1 = *(const u32x4*)(sc + 4);
    unsigned T = min(min(min(g0[0], g0[1]), min(g0[2], g0[3])), min(min(g1[0], g1[1]), min(g1[2], g1[3])));
    unsigned pos[8], tot = 0;
#pragma unroll
    for (int i = 0; i < 8; ++i) {
      unsigned k = i < 4 ? ka[i & 3] : kb[i & 3];
      unsigned long long bm = __ballot(k >= T);
      pos[i] = tot + __builtin_amdgcn_mbcnt_hi((unsigned)(bm >> 32), __builtin_amdgcn_mbcnt_lo((unsigned)bm, 0));
      tot += __popcll(bm);
    }
    unsigned base = 0;
    if (lane == 0) base = atomicAdd(&sc[8], tot);
    base = __builtin_amdgcn_readfirstlane(base);
#pragma unroll
    for (int i = 0; i < 8; ++i) {
      unsigned k = i < 4 ? ka[i & 3] : kb[i & 3];
      if (k >= T) sc[32 + base + pos[i]] = k;
    }
  }
  lds_bar();
  const int n = sc[8];
  for (int i = tid; i < n; i += NT) {
    unsigned me = sc[32 + i];
    int rank = 0;
    for (int j = 0; j < n; j += 4) {
      u32x4 v = *(const u32x4*)(sc + 32 + j);
      rank += (v[0] > me) + (j + 1 < n && v[1] > me) + (j + 2 < n && v[2] > me) + (j + 3 < n && v[3] > me);
    }
    if (rank < 8) sc[16 + rank] = me;
  }
  lds_bar();
}

template <int N> DEV void poll16(u32x4 (&v)[N], rsrc_t r, int voff, int soff, int step) {
  for (int sp = 0; sp < SPIN_CAP; ++sp) {
    bool bad = false;
#pragma unroll
    for (int i = 0; i < N; ++i) v[i] = ld16c(r, voff, soff + i * step);
#pragma unroll
    for (int i = 0; i < N; ++i) bad |= has_sent(v[i]);
    if (!__any(bad)) break;
    __builtin_amdgcn_s_sleep(1);
    asm volatile("" ::: "memory");
  }
}

DEV void ldunit(unsigned (&r)[8], rsrc_t rs, int voff, int soff, int ld) {
#pragma unroll
  for (int i = 0; i < 8; ++i) r[i] = ld4(rs, voff, soff + i * ld);
}
DEV void trunit(const unsigned (&r)[8], u32x4& f0, u32x4& f1) {
#pragma unroll
  for (int i = 0; i < 4; ++i) {
    f0[i] = __builtin_amdgcn_perm(r[2 * i + 1], r[2 * i], 0x05040100);
    f1[i] = __builtin_amdgcn_perm(r[2 * i + 1], r[2 * i], 0x07060302);
  }
}
#endif

constexpr int G1_SLOTS = 7, G1_STAGE = 20480;
constexpr int G1_HS = G1_SLOTS * G1_STAGE;
constexpr int G1_W = G1_HS + 1024;
constexpr int G1_LDS = G1_W + 2048;

template <bool FIRST>
__global__ __launch_bounds__(512) void k_gemm1(Params p) {
#if defined(__HIP_DEVICE_COMPILE__)
  extern __shared__ __attribute__((aligned(16))) char smem[];
  const int tid = threadIdx.x, lane = tid & 63;
  const int w = __builtin_amdgcn_readfirstlane(tid >> 6);
  const int b = blockIdx.x, xcd = b & 7, j = b >> 3;
  const int half = xcd & 1, kq = (xcd >> 1) & 1, xm2 = xcd >> 2;
  const int mt = (kq * 2 + xm2) * 8 + (j & 7), ntl = j >> 3;
  const int fr = lane & 15, fq = lane >> 4;
  const int offH = (fr >> 3) * 1024 + (fr & 7) * 128 + ((fq ^ (fr & 7)) << 4);
  if (w >= 4) {
    const int lw = w - 4;
    rsrc_t rH = mk((half ? p.Hi : p.Hr) + ntl * 160 * 640, 160 * 1280);
    const int r7 = lane >> 3;
    const int voff = (lw * 8 + r7) * 1280 + (((lane & 7) ^ r7) << 4);
    auto issue = [&](int g, int slot) {
#pragma unroll
      for (int i = 0; i < 5; ++i) glds(rH, smem + slot * G1_STAGE + (i * 4 + lw) * 1024, voff, g * 128 + i * 32 * 1280);
    };
    if (FIRST) {
      if (lw == 0 && lane < 32) *(u32x4*)(p.ws + WS_HO + (b * 32 + lane) * 16) = u32x4{SENT, SENT, SENT, SENT};
      wait_vm<0>();
    } else if (lw < 2) {
      glds(mk(p.wr, 1280), smem + G1_W + lw * 1024, lane * 16 + lw * 1024, 0);
    }
#pragma unroll
    for (int g = 0; g < 7; ++g) issue(g, g);
    f32x4 hs[3] = {};
    const u32x4 ones = {0x3f803f80u, 0x3f803f80u, 0x3f803f80u, 0x3f803f80u};
    auto hsum = [&](int g) {
      const char* base = smem + (g % 7) * G1_STAGE + lw * 2048;
#pragma unroll
      for (int par = 0; par < 2; ++par) {
        const char* q = base + (offH ^ (par << 6));
        hs[0] = mfma(ones, *(const u32x4*)q, hs[0]);
        hs[1] = mfma(ones, *(const u32x4*)(q + 8192), hs[1]);
        if (lw < 2) hs[2] = mfma(ones, *(const u32x4*)(q + 16384), hs[2]);
      }
      wait_lgkm0();
    };
    wait_vm<30>(); bar(); hsum(0);
    wait_vm<25>(); bar(); issue(7, 0); hsum(1);
    wait_vm<25>(); bar(); issue(8, 1); hsum(2);
    wait_vm<25>(); bar(); issue(9, 2); hsum(3);
    wait_vm<25>(); bar(); hsum(4);
    wait_vm<20>(); bar(); hsum(5);
    wait_vm<15>(); bar(); hsum(6);
    wait_vm<10>(); bar(); hsum(7);
    wait_vm<5>(); bar(); hsum(8);
    wait_vm<0>(); bar(); hsum(9);
    if (fq == 0) {
      float* h = (float*)(smem + G1_HS);
      h[lw * 16 + fr] = hs[0][0];
      h[(lw + 4) * 16 + fr] = hs[1][0];
      if (lw < 2) h[(lw + 8) * 16 + fr] = hs[2][0];
    }
    lds_bar();
  } else {
    constexpr int NA = FIRST ? 1 : 4, WIN = FIRST ? 10 : 4;
    const int t16 = mt * 4 + w, s0 = t16 * 16;
    const int u = half * 4 + ntl;
    rsrc_t rX = FIRST ? mk(p.x0, SZ_X) : mk(p.ws + WS_P1, 4 * SZ_X);
    const int voffX = FIRST ? (s0 + fr) * 1280 + fq * 16 : lane * 16;
    u32x4 raw[WIN][NA];
    auto ldx = [&](int c) {
#pragma unroll
      for (int a = 0; a < NA; ++a) raw[c % WIN][a] = FIRST ? ld16(rX, voffX, c * 64) : ld16(rX, voffX, a * SZ_X + (t16 * 20 + c) * 1024);
    };
#pragma unroll
    for (int c = 0; c < WIN; ++c) ldx(c);
    f32x4 acc[10] = {};
    f32x2 sum = {0.f, 0.f}, sq = {0.f, 0.f};
    float dw = 0.f, sw = 0.f;
    u32x4 hf[2][10], wf[2], xf[2];
    auto rdh = [&](int c) {
      const char* q = smem + ((c >> 1) % 7) * G1_STAGE + (offH ^ ((c & 1) << 6));
#pragma unroll
      for (int n = 0; n < 10; ++n) hf[c & 1][n] = *(const u32x4*)(q + n * 2048);
      if (!FIRST) wf[c & 1] = *(const u32x4*)(smem + G1_W + c * 64 + fq * 16);
    };
    auto prep = [&](int c) {
      u32x4 o;
#pragma unroll
      for (int i = 0; i < 4; ++i) {
        f32x2 v = {lo(raw[c % WIN][0][i]), hi(raw[c % WIN][0][i])};
        if (!FIRST) {
#pragma unroll
          for (int a = 1; a < NA; ++a) v += f32x2{lo(raw[c % WIN][a][i]), hi(raw[c % WIN][a][i])};
          o[i] = pk2(v[0], v[1]);
        } else {
          o[i] = raw[c % WIN][0][i];
        }
        sum += v;
        sq += v * v;
      }
      asm volatile("" : "+v"(sum), "+v"(sq), "+v"(o));
      xf[c & 1] = o;
      if (c + WIN < 20) ldx(c + WIN);
      if (!FIRST && (c & 7) == u) *(u32x4*)(p.ws + WS_X1 + (s0 + fr) * 1280 + c * 64 + fq * 16) = o;
    };
    bar();
    rdh(0);
    prep(0);
#pragma unroll
    for (int c = 0; c < 20; ++c) {
      wait_lgkm0();
      if ((c & 1) && c < 19) bar();
      __builtin_amdgcn_sched_barrier(0);
      if (c < 19) { rdh(c + 1); prep(c + 1); }
#pragma unroll
      for (int n = 0; n < 10; ++n) acc[n] = mfma(xf[c & 1], hf[c & 1][n], acc[n]);
      if (!FIRST) {
#pragma unroll
        for (int i = 0; i < 4; ++i) { dw = dot2(xf[c & 1][i], wf[c & 1][i], dw); sw = dot2(wf[c & 1][i], 0x3f803f80u, sw); }
        asm volatile("" : "+v"(dw), "+v"(sw));
      }
      __builtin_amdgcn_sched_barrier(0);
    }
    const float mean = xq_sum(sum[0] + sum[1]) * (1.f / 640.f);
    const float var = fmaxf(xq_sum(sq[0] + sq[1]) * (1.f / 640.f) - mean * mean, 0.f);
    float scale = __builtin_amdgcn_rsqf(var + EPS);
    if (!FIRST) {
      const float g1 = 1.f + scale;
      scale = g1 * __builtin_amdgcn_rsqf(g1 * g1 * var + EPS);
      dw = xq_sum(dw); sw = xq_sum(sw);
      if (u == 0 && fq == 0) {
        const float mb = rb(mean), rs = 1.f / rb(__builtin_sqrtf(rb(rb(var) + EPS)));
        *(f32x4*)(p.ws + WS_ST + (s0 + fr) * 16) = f32x4{mb, rs, mean, scale};
        *(unsigned*)(p.ws + WS_KEY1 + (s0 + fr) * 4) = mkkey((dw - mb * sw) * rs, s0 + fr);
      }
    }
    float mi[4], si[4];
#pragma unroll
    for (int i = 0; i < 4; ++i) { mi[i] = __shfl(mean, fq * 4 + i); si[i] = __shfl(scale, fq * 4 + i); }
    lds_bar();
    char* ab = p.ws + (FIRST ? WS_AB1 : WS_AB3) + ((half * 8 + ntl * 2) * 64 + (t16 >> 1)) * 5120 + ((t16 & 1) * 2 + (fq >> 1)) * 256 + fr * 16 + (fq & 1) * 8;
#pragma unroll
    for (int n = 0; n < 10; ++n) {
      const float h = *(const float*)(smem + G1_HS + (n * 16 + fr) * 4);
      u32x2 v = {pk2(si[0] * (acc[n][0] - mi[0] * h), si[1] * (acc[n][1] - mi[1] * h)), pk2(si[2] * (acc[n][2] - mi[2] * h), si[3] * (acc[n][3] - mi[3] * h))};
      *(u32x2*)(ab + (n / 5) * (64 * 5120) + (n % 5) * 1024) = v;
    }
  }
#endif
}

#if defined(__HIP_DEVICE_COMPILE__)
constexpr int FL_TOP = 0;
constexpr int FL_XS = 8448;
constexpr int FL_RED = 18688;
constexpr int FL_HB = FL_RED + 12288;

template <bool L2> DEV void ffn_up(const Params& p, char* smem, int g) {
  const int tid = threadIdx.x, lane = tid & 63, fr = lane & 15, fq = lane >> 4;
  const int w = __builtin_amdgcn_readfirstlane(tid >> 6);
  rsrc_t rHO = mk(p.ws + WS_HO, HO_BYTES);
  u32x4 ka = {}, kb = {};
  if (L2 && tid < 256) { ka = *(const u32x4*)(p.ws + WS_KEY1 + tid * 32); kb = *(const u32x4*)(p.ws + WS_KEY1 + tid * 32 + 16); }
  unsigned wgt[8][8];
  if (w < 5) {
    rsrc_t rg = mk(p.wg, 640 * 3456), ru = mk(p.wu, 640 * 3456);
    const int voff = fq * 8 * 3456 + g * 64 + fr * 4;
#pragma unroll
    for (int i = 0; i < 4; ++i) { ldunit(wgt[2 * i], rg, voff, (w * 4 + i) * 32 * 3456, 3456); ldunit(wgt[2 * i + 1], ru, voff, (w * 4 + i) * 32 * 3456, 3456); }
  }
  u32x4 xa[4];
  unsigned cs = 0;
  if (L2) {
    unsigned* sc = (unsigned*)(smem + FL_TOP);
    top8<384>(ka, kb, sc, tid);
    if (w == 5) {
      u32x4 v[10]; f32x2 st[10];
#pragma unroll
      for (int i = 0; i < 10; ++i) {
        const int pc = i * 64 + lane, tok = pc / 80, piece = pc % 80, row = key_row(sc[16 + tok]);
        v[i] = *(const u32x4*)(p.ws + WS_X1 + row * 1280 + piece * 16);
        st[i] = *(const f32x2*)(p.ws + WS_ST + row * 16);
      }
#pragma unroll
      for (int i = 0; i < 10; ++i) {
        const int pc = i * 64 + lane, tok = pc / 80, piece = pc % 80;
        u32x4 o;
#pragma unroll
        for (int e = 0; e < 4; ++e) o[e] = pk2(rb(lo(v[i][e]) - st[i][0]) * st[i][1], rb(hi(v[i][e]) - st[i][0]) * st[i][1]);
        *(u32x4*)(smem + FL_XS + piece * 128 + tok * 16) = o;
      }
    } else if (tid < 304) {
      const int t = min(g * 38 + (tid >> 3), 2047), jp = tid & 7;
      const u16* C = (jp < 4 ? p.Cr : p.Ci) + t * 2048;
      cs = (unsigned)C[key_row(sc[16 + (jp & 3) * 2])] | ((unsigned)C[key_row(sc[17 + (jp & 3) * 2])] << 16);
    }
    lds_bar();
    if (w < 5) {
#pragma unroll
      for (int i = 0; i < 4; ++i) xa[i] = *(const u32x4*)(smem + FL_XS + (w * 4 + i) * 512 + fq * 128 + (fr & 7) * 16);
    }
  } else if (w < 5) {
    poll16<4>(xa, rHO, fq * 128 + (fr & 7) * 16, HO_XS3 + w * 2048, 512);
  }
  float* red = (float*)(smem + FL_RED);
  if (w < 5) {
    f32x4 acc[4] = {};
#pragma unroll
    for (int i = 0; i < 4; ++i) {
      u32x4 f0, f1;
      trunit(wgt[2 * i], f0, f1);
      acc[0] = mfma(xa[i], f0, acc[0]); acc[1] = mfma(xa[i], f1, acc[1]);
      trunit(wgt[2 * i + 1], f0, f1);
      acc[2] = mfma(xa[i], f0, acc[2]); acc[3] = mfma(xa[i], f1, acc[3]);
    }
    if (fq < 2) {
#pragma unroll
      for (int a = 0; a < 4; ++a) *(f32x4*)(red + ((w * 4 + a) * 32 + lane) * 4) = acc[a];
    }
  }
  lds_bar();
  if (tid < 256) {
    const int tok = tid >> 5, col = tid & 31;
    const int o = ((col & 1) * 32 + (tok >> 2) * 16 + (col >> 1)) * 4 + (tok & 3);
    float gs = 0.f, us = 0.f;
#pragma unroll
    for (int q = 0; q < 5; ++q) { gs += red[q * 512 + o]; us += red[q * 512 + 256 + o]; }
    gs = rb(gs); us = rb(us);
    const float h = rb(gs * sigm(gs)) * us;
    *(u16*)(smem + FL_HB + (col >> 3) * 128 + tok * 16 + (col & 7) * 2) = (u16)pk2(h, h);
  }
  lds_bar();
  if (tid < 32) {
    u32x4 v = *(const u32x4*)(smem + FL_HB + tid * 16);
#pragma unroll
    for (int e = 0; e < 4; ++e) v[e] = nosent(v[e]);
    __builtin_amdgcn_raw_buffer_store_b128(v, rHO, tid * 16, (L2 ? HO_H2 : HO_H4) + g * 512, 16);
  }
  if (L2 && tid < 304 && g * 38 + (tid >> 3) < 2048) *(unsigned*)(p.ws + WS_CS + (g * 38 + (tid >> 3)) * 32 + (tid & 7) * 4) = cs;
}

template <bool L2> DEV void ffn_down(const Params& p, char* smem, int g) {
  const int tid = threadIdx.x, lane = tid & 63, fr = lane & 15, fq = lane >> 4;
  const int w = __builtin_amdgcn_readfirstlane(tid >> 6);
  rsrc_t rHO = mk(p.ws + WS_HO, HO_BYTES);
  u32x4 ka = {}, kb = {};
  if (L2 && tid < 256) { ka = *(const u32x4*)(p.ws + WS_KEY1 + tid * 32); kb = *(const u32x4*)(p.ws + WS_KEY1 + tid * 32 + 16); }
  unsigned wgt[9][8];
  {
    rsrc_t rd = mk(p.wd, 1728 * 1280);
    const int voff = fq * 8 * 1280 + g * 64 + fr * 4;
#pragma unroll
    for (int i = 0; i < 9; ++i) ldunit(wgt[i], rd, voff, (w * 9 + i) * 32 * 1280, 1280);
  }
  const int tok = (tid >> 4) & 7, cp = tid & 15;
  const int po = g * 512 + (cp >> 2) * 128 + tok * 16 + (cp & 3) * 4;
  unsigned key = 0, xr = 0, xn = 0;
  f32x4 st = {};
  if (L2) {
    unsigned* sc = (unsigned*)(smem + FL_TOP);
    top8<384>(ka, kb, sc, tid);
    if (tid < 128) {
      key = sc[16 + tok];
      xr = *(const unsigned*)(p.ws + WS_X1 + key_row(key) * 1280 + g * 64 + cp * 4);
      st = *(const f32x4*)(p.ws + WS_ST + key_row(key) * 16);
    }
  } else if (tid < 128) {
    for (int sp = 0; sp < SPIN_CAP; ++sp) {
      key = ld4c(rHO, tok * 4, HO_KS3); xr = ld4c(rHO, po, HO_XR3); xn = ld4c(rHO, po, HO_XS3);
      if (!__any(key == SENT || xr == SENT || xn == SENT)) break;
      __builtin_amdgcn_s_sleep(2);
      asm volatile("" ::: "memory");
    }
  }
  u32x4 f0[9], f1[9];
#pragma unroll
  for (int i = 0; i < 9; ++i) trunit(wgt[i], f0[i], f1[i]);
  u32x4 ha[9];
  poll16<9>(ha, rHO, fq * 128 + (fr & 7) * 16, (L2 ? HO_H2 : HO_H4) + w * 9 * 512, 512);
  f32x4 a0 = {}, a1 = {};
#pragma unroll
  for (int i = 0; i < 9; ++i) { a0 = mfma(ha[i], f0[i], a0); a1 = mfma(ha[i], f1[i], a1); }
  float* red = (float*)(smem + FL_RED);
  if (fq < 2) { *(f32x4*)(red + ((w * 2) * 32 + lane) * 4) = a0; *(f32x4*)(red + ((w * 2 + 1) * 32 + lane) * 4) = a1; }
  lds_bar();
  if (tid < 128) {
    const int o = ((tok >> 2) * 16 + cp) * 4 + (tok & 3);
    float y0 = 0.f, y1 = 0.f;
#pragma unroll
    for (int q = 0; q < 6; ++q) { y0 += red[q * 256 + o]; y1 += red[q * 256 + 128 + o]; }
    const float sg = rb(sigm(key_val(key)));
    if (L2) xn = pk2(rb(lo(xr) - st[0]) * st[1], rb(hi(xr) - st[0]) * st[1]);
    const unsigned res = pk2(lo(xr) + rb(lo(xn) + rb(rb(y0) * sg)), hi(xr) + rb(hi(xn) + rb(rb(y1) * sg)));
    if (L2) {
      __builtin_amdgcn_raw_buffer_store_b32(nosent(res), rHO, po, HO_YN, 16);
      __builtin_amdgcn_raw_buffer_store_b32(nosent(pk2((lo(xr) - st[2]) * st[3], (hi(xr) - st[2]) * st[3])), rHO, po, HO_YB, 16);
      if (g == 0 && cp == 0) __builtin_amdgcn_raw_buffer_store_b32(key, rHO, tok * 4, HO_IDX2, 16);
    } else {
      *(unsigned*)((char*)p.out + key_row(key) * 1280 + g * 64 + cp * 4) = res;
    }
  }
}

DEV void ffn_fix(const Params& p, char* smem, int g) {
  const int tid = threadIdx.x, lane = tid & 63, fr = lane & 15, fq = lane >> 4;
  const int w = __builtin_amdgcn_readfirstlane(tid >> 6);
  rsrc_t rHO = mk(p.ws + WS_HO, HO_BYTES);
  const int half = g / 40, e0 = (g % 40) * 16;
  float* red = (float*)(smem + FL_RED);
  u32x4 hf[4], xa[4];
  if (w < 5) {
    rsrc_t rH = mk(half ? p.Hi : p.Hr, 640 * 1280);
#pragma unroll
    for (int i = 0; i < 4; ++i) hf[i] = ld16(rH, (e0 + fr) * 1280 + fq * 16, (w * 4 + i) * 64);
    poll16<4>(xa, rHO, (fr < 8 ? HO_YN : HO_YB) + fq * 128 + (fr & 7) * 16, w * 2048, 512);
    float s = 0.f, q = 0.f;
#pragma unroll
    for (int i = 0; i < 4; ++i)
#pragma unroll
      for (int e = 0; e < 4; ++e) { float a = lo(xa[i][e]), c = hi(xa[i][e]); s += a + c; q += a * a + c * c; }
    s = xq_sum(s); q = xq_sum(q);
    if (fq == 0) *(f32x2*)(red + (w * 16 + fr) * 2) = f32x2{s, q};
  }
  lds_bar();
  if (w < 5) {
    f32x2 t = {0.f, 0.f};
#pragma unroll
    for (int q = 0; q < 5; ++q) t += *(const f32x2*)(red + (q * 16 + fr) * 2);
    const float mean = t[0] * (1.f / 640.f), var = fmaxf(t[1] * (1.f / 640.f) - mean * mean, 0.f);
    const float mb = rb(mean), rs = 1.f / rb(__builtin_sqrtf(rb(rb(var) + EPS)));
    if (g == 0) {
      unsigned key = SENT;
      for (int sp = 0; sp < SPIN_CAP; ++sp) {
        key = ld4c(rHO, (fr & 7) * 4, HO_IDX2);
        if (!__any(key == SENT)) break;
        __builtin_amdgcn_s_sleep(1);
        asm volatile("" ::: "memory");
      }
      if (fr < 8) {
#pragma unroll
        for (int i = 0; i < 4; ++i) *(u32x4*)(p.ws + WS_X1 + key_row(key) * 1280 + (w * 4 + i) * 64 + fq * 16) = xa[i];
        if (w == 0 && fq == 0) *(f32x2*)(p.ws + WS_ST + key_row(key) * 16) = f32x2{0.f, 0.f};
      }
    }
    f32x4 acc = {};
#pragma unroll
    for (int i = 0; i < 4; ++i) {
      u32x4 o;
#pragma unroll
      for (int e = 0; e < 4; ++e) {
        const unsigned nv = pk2(rb(lo(xa[i][e]) - mb) * rs, rb(hi(xa[i][e]) - mb) * rs);
        o[e] = fr < 8 ? nv : xa[i][e];
      }
      acc = mfma(o, hf[i], acc);
    }
    *(f32x4*)(red + 256 + (w * 64 + lane) * 4) = acc;
  }
  lds_bar();
  if (tid < 64) {
    f32x4 a = {};
#pragma unroll
    for (int q = 0; q < 5; ++q) a += *(const f32x4*)(red + 256 + (q * 64 + lane) * 4);
    *(u32x2*)(p.ws + WS_T + (e0 + fr) * 64 + half * 32 + fq * 8) = u32x2{pk2(a[0], a[1]), pk2(a[2], a[3])};
  }
}
#endif

constexpr int G2_B = 3 * 16384;
constexpr int G2_LDS = G2_B + 3 * 10240;
constexpr int NG_UP = 54, NG_DOWN = 20, NG_FIX = 80;

template <bool FIRST>
__global__ __launch_bounds__(384, FIRST ? 2 : 3) void k_gemm2(Params p, int role0, int unused) {
#if defined(__HIP_DEVICE_COMPILE__)
  extern __shared__ __attribute__((aligned(16))) char smem[];
  const int b = blockIdx.x + role0;
  if (!FIRST && b >= 256) {
    const int g = b - 256;
    if (g < NG_UP) ffn_up<true>(p, smem, g);
    else if (g < NG_UP + NG_DOWN) ffn_down<true>(p, smem, g - NG_UP);
    else ffn_fix(p, smem, g - NG_UP - NG_DOWN);
    return;
  }
  const int tid = threadIdx.x, lane = tid & 63;
  const int w = __builtin_amdgcn_readfirstlane(tid >> 6);
  const int xcd = b & 7, j = b >> 3;
  const int half = xcd & 1, kq = (xcd >> 1) & 1, mt = (xcd >> 2) * 8 + (j & 7), nt = j >> 3;
  if (w >= 4) {
    const int lw = w - 4;
    rsrc_t rA = mk((half ? p.Ci : p.Cr) + mt * 128 * 2048 + kq * 1024, 128 * 4096 - kq * 2048);
    rsrc_t rB = mk(p.ws + (FIRST ? WS_AB1 : WS_AB3) + ((half * 8 + nt * 2 + lw) * 64 + kq * 32) * 5120, 32 * 5120);
    const int r7 = lane >> 3;
    const int voffA = (lw * 8 + r7) * 4096 + (((lane & 7) ^ r7) << 4);
    const int voffB = lane * 16;
    auto issueA = [&](int t, int slot) {
#pragma unroll
      for (int i = 0; i < 8; ++i) glds(rA, smem + slot * 16384 + (i * 2 + lw) * 1024, voffA, t * 128 + i * 16 * 4096);
    };
    auto issueB = [&](int n, int slot) {
#pragma unroll
      for (int i = 0; i < 5; ++i) glds(rB, smem + G2_B + slot * 10240 + lw * 5120 + i * 1024, voffB, n * 5120 + i * 1024);
    };
    issueA(0, 0); issueB(0, 0); issueB(1, 1); issueA(1, 1); issueB(2, 2); issueA(2, 2);
    wait_vm<26>(); bar();
    wait_vm<21>(); bar(); issueB(3, 0);
    wait_vm<13>(); bar(); issueB(4, 1); issueA(3, 0);
    for (int n = 2; n < 26; n += 6) {
      const int t = n >> 1;
      wait_vm<13>(); bar(); issueB(n + 3, 2);
      wait_vm<13>(); bar(); issueB(n + 4, 0); issueA(t + 3, 1);
      wait_vm<13>(); bar(); issueB(n + 5, 1);
      wait_vm<13>(); bar(); issueB(n + 6, 2); issueA(t + 4, 2);
      wait_vm<13>(); bar(); issueB(n + 7, 0);
      wait_vm<13>(); bar(); issueB(n + 8, 1); issueA(t + 5, 0);
    }
    wait_vm<13>(); bar(); issueB(29, 2);
    wait_vm<13>(); bar(); issueB(30, 0);
    wait_vm<5>(); bar(); issueB(31, 1);
    wait_vm<5>(); bar();
    wait_vm<0>(); bar();
  } else {
    const int fr = lane & 15, fq = lane >> 4;
    const int wm = w & 1, wn = w >> 1;
    const int offC = wm * 8192 + (fr >> 3) * 1024 + (fr & 7) * 128 + ((fq ^ (fr & 7)) << 4);
    const int offB = G2_B + wn * 5120 + lane * 16;
    f32x4 acc[4][5] = {};
    u32x4 xc[4], xb[5], yc[4], yb[5];
    auto rd = [&](u32x4 (&c)[4], u32x4 (&bb)[5], int sa, int sb, int h) {
      const char* pc = smem + sa * 16384 + (offC ^ (h << 6));
      const char* pb = smem + sb * 10240 + offB;
#pragma unroll
      for (int i = 0; i < 4; ++i) c[i] = *(const u32x4*)(pc + i * 2048);
#pragma unroll
      for (int n = 0; n < 5; ++n) bb[n] = *(const u32x4*)(pb + n * 1024);
    };
    auto mm = [&](u32x4 (&c)[4], u32x4 (&bb)[5]) {
#pragma unroll
      for (int n = 0; n < 5; ++n)
#pragma unroll
        for (int i = 0; i < 4; ++i) acc[i][n] = mfma(bb[n], c[i], acc[i][n]);
    };
    auto sched = [&]() {
#pragma unroll
      for (int q = 0; q < 9; ++q) { __builtin_amdgcn_sched_group_barrier(0x8, 1, 0); __builtin_amdgcn_sched_group_barrier(0x100, 1, 0); }
      __builtin_amdgcn_sched_group_barrier(0x8, 11, 0);
    };
    bar();
    rd(xc, xb, 0, 0, 0);
    int sa = 0, sb = 0;
    for (int t = 0; t < 15; ++t) {
      sb = sb == 2 ? 0 : sb + 1;
      wait_lgkm0(); bar();
      __builtin_amdgcn_sched_barrier(0);
      rd(yc, yb, sa, sb, 1); mm(xc, xb); sched();
      __builtin_amdgcn_sched_barrier(0);
      sb = sb == 2 ? 0 : sb + 1;
      sa = sa == 2 ? 0 : sa + 1;
      wait_lgkm0(); bar();
      __builtin_amdgcn_sched_barrier(0);
      rd(xc, xb, sa, sb, 0); mm(yc, yb); sched();
      __builtin_amdgcn_sched_barrier(0);
    }
    wait_lgkm0(); bar();
    const bool addx = FIRST && half == 0 && kq == 0;
    const int t16 = mt * 8 + wm * 4, e16 = (nt * 2 + wn) * 5;
    u32x2 xr[4][5] = {};
    if (addx) {
      rsrc_t rX = mk(p.x0, SZ_X);
#pragma unroll
      for (int i = 0; i < 4; ++i)
#pragma unroll
        for (int n = 0; n < 5; ++n) xr[i][n] = __builtin_amdgcn_raw_buffer_load_b64(rX, fr * 1280 + fq * 8, (t16 + i) * 20480 + (e16 + n) * 32, 0);
    }
    rd(yc, yb, 0, 1, 1); mm(xc, xb); mm(yc, yb);
    const float sgn = half ? -1.f : 1.f;
    char* P = p.ws + (FIRST ? WS_P1 : WS_P3) + (half * 2 + kq) * SZ_X + (fq >> 1) * 256 + fr * 16 + (fq & 1) * 8;
#pragma unroll
    for (int i = 0; i < 4; ++i)
#pragma unroll
      for (int n = 0; n < 5; ++n) {
        const f32x4 a = acc[i][n];
        u32x2 v = {pk2(sgn * a[0] + lo(xr[i][n][0]), sgn * a[1] + hi(xr[i][n][0])), pk2(sgn * a[2] + lo(xr[i][n][1]), sgn * a[3] + hi(xr[i][n][1]))};
        *(u32x2*)(P + ((t16 + i) * 20 + ((e16 + n) >> 1)) * 1024 + ((e16 + n) & 1) * 512) = v;
      }
  }
#endif
}

constexpr int FE_LDS = 32768;

__global__ __launch_bounds__(384) void k_final(Params p, int role0, int unused) {
#if defined(__HIP_DEVICE_COMPILE__)
  extern __shared__ __attribute__((aligned(16))) char smem[];
  const int b = blockIdx.x + role0;
  if (b >= 128) {
    const int g = b - 128;
    if (g < NG_UP) ffn_up<false>(p, smem, g);
    else ffn_down<false>(p, smem, g - NG_UP);
    return;
  }
  const int tid = threadIdx.x, lane = tid & 63, fr = lane & 15, fq = lane >> 4;
  const int w = __builtin_amdgcn_readfirstlane(tid >> 6);
  rsrc_t rHO = mk(p.ws + WS_HO, HO_BYTES);
  const int t = b * 16 + fr;
  float* red = (float*)(smem + FL_RED);
  u32x4 x3[4];
  if (w < 5) {
    rsrc_t rP = mk(p.ws + WS_P3, 4 * SZ_X), rT = mk(p.ws + WS_T, 40960), rX = mk(p.ws + WS_X1, SZ_X), rW = mk(p.wr, 1280);
    u32x4 tf[4][2], pp[4][4], x1[4], wf[4];
    u32x4 cs = *(const u32x4*)(p.ws + WS_CS + t * 32 + (fq >> 1) * 16);
#pragma unroll
    for (int i = 0; i < 4; ++i)
#pragma unroll
      for (int h = 0; h < 2; ++h) tf[i][h] = ld16(rT, (8 * (fr >> 2) + (fr & 3)) * 64 + fq * 16, ((w * 4 + i) * 32 + 4 * h) * 64);
    const f32x2 st = *(const f32x2*)(p.ws + WS_ST + t * 16);
#pragma unroll
    for (int i = 0; i < 4; ++i) {
#pragma unroll
      for (int a = 0; a < 4; ++a) pp[i][a] = ld16(rP, lane * 16, a * SZ_X + (b * 20 + w * 4 + i) * 1024);
      x1[i] = ld16(rX, t * 1280 + fq * 16, (w * 4 + i) * 64);
      wf[i] = ld16(rW, fq * 16, (w * 4 + i) * 64);
    }
    const unsigned flip = (fq == 1 || fq == 2) ? 0x80008000u : 0u;
#pragma unroll
    for (int e = 0; e < 4; ++e) cs[e] ^= flip;
    float s = 0.f, q = 0.f, dw = 0.f, sw = 0.f;
#pragma unroll
    for (int i = 0; i < 4; ++i) {
      const f32x4 z = {};
      const f32x4 c0 = mfma(tf[i][0], cs, z), c1 = mfma(tf[i][1], cs, z);
#pragma unroll
      for (int e = 0; e < 4; ++e) {
        float y0 = e < 2 ? c0[2 * e] : c1[2 * e - 4], y1 = e < 2 ? c0[2 * e + 1] : c1[2 * e - 3];
#pragma unroll
        for (int a = 0; a < 4; ++a) { y0 += lo(pp[i][a][e]); y1 += hi(pp[i][a][e]); }
        const float a0 = lo(x1[i][e]), a1 = hi(x1[i][e]);
        const float b0 = rb(a0 + rb(rb(a0 - st[0]) * st[1])), b1 = rb(a1 + rb(rb(a1 - st[0]) * st[1]));
        const unsigned v = pk2(b0 + rb(y0), b1 + rb(y1));
        x3[i][e] = v;
        s += lo(v) + hi(v); q += lo(v) * lo(v) + hi(v) * hi(v);
        dw = dot2(v, wf[i][e], dw); sw = dot2(wf[i][e], 0x3f803f80u, sw);
      }
    }
    s = xq_sum(s); q = xq_sum(q); dw = xq_sum(dw); sw = xq_sum(sw);
    if (fq == 0) *(f32x4*)(red + (w * 16 + fr) * 4) = f32x4{s, q, dw, sw};
  }
  lds_bar();
  f32x4 tt = {};
#pragma unroll
  for (int q = 0; q < 5; ++q) tt += *(const f32x4*)(red + (q * 16 + fr) * 4);
  const float mean = tt[0] * (1.f / 640.f), var = fmaxf(tt[1] * (1.f / 640.f) - mean * mean, 0.f);
  const float mb = rb(mean), rs = 1.f / rb(__builtin_sqrtf(rb(rb(var) + EPS)));
  const unsigned key = mkkey((tt[2] - mb * tt[3]) * rs, t);
  if (tid < 16) __builtin_amdgcn_raw_buffer_store_b32(key, rHO, t * 4, HO_KEY3, 16);
  u32x4 kk[2] = {};
  if (w < 4) poll16<2>(kk, rHO, tid * 32, HO_KEY3, 16);
  unsigned* sc = (unsigned*)(smem + FL_TOP);
  top8<384>(kk[0], kk[1], sc, tid);
  if (w < 5) {
    const u32x4 s0 = *(const u32x4*)(sc + 16), s1 = *(const u32x4*)(sc + 20);
    int slot = -1;
#pragma unroll
    for (int e = 0; e < 4; ++e) { if (s0[e] == key) slot = e; if (s1[e] == key) slot = 4 + e; }
#pragma unroll
    for (int i = 0; i < 4; ++i) {
      u32x4 xn, o, xr;
#pragma unroll
      for (int e = 0; e < 4; ++e) {
        const unsigned v = x3[i][e];
        xn[e] = pk2(rb(lo(v) - mb) * rs, rb(hi(v) - mb) * rs);
        o[e] = pk2(lo(v) + lo(xn[e]), hi(v) + hi(xn[e]));
        xn[e] = nosent(xn[e]); xr[e] = nosent(v);
      }
      if (slot >= 0) {
        __builtin_amdgcn_raw_buffer_store_b128(xn, rHO, fq * 128 + slot * 16, HO_XS3 + (w * 4 + i) * 512, 16);
        __builtin_amdgcn_raw_buffer_store_b128(xr, rHO, fq * 128 + slot * 16, HO_XR3 + (w * 4 + i) * 512, 16);
      } else {
        *(u32x4*)((char*)p.out + t * 1280 + (w * 4 + i) * 64 + fq * 16) = o;
      }
    }
    if (slot >= 0 && tid < 16) __builtin_amdgcn_raw_buffer_store_b32(key, rHO, slot * 4, HO_KS3, 16);
  }
#endif
}

extern "C" void kernel_launch(void* const* d_in, const int* in_sizes, int n_in, void* d_out, int out_size, void* d_ws, size_t ws_size,
                              hipStream_t stream) {
  Params p;
  p.x0 = (const u16*)d_in[0]; p.Cr = (const u16*)d_in[1]; p.Ci = (const u16*)d_in[2]; p.Hr = (const u16*)d_in[3]; p.Hi = (const u16*)d_in[4];
  p.wr = (const u16*)d_in[6]; p.wg = (const u16*)d_in[7]; p.wu = (const u16*)d_in[8]; p.wd = (const u16*)d_in[9];
  p.out = (u16*)d_out; p.ws = (char*)d_ws;
  (void)hipFuncSetAttribute((const void*)k_gemm1<true>, hipFuncAttributeMaxDynamicSharedMemorySize, G1_LDS);
  (void)hipFuncSetAttribute((const void*)k_gemm1<false>, hipFuncAttributeMaxDynamicSharedMemorySize, G1_LDS);
  (void)hipFuncSetAttribute((const void*)k_gemm2<true>, hipFuncAttributeMaxDynamicSharedMemorySize, G2_LDS);
  (void)hipFuncSetAttribute((const void*)k_gemm2<false>, hipFuncAttributeMaxDynamicSharedMemorySize, G2_LDS);
  k_gemm1<true><<<256, 512, G1_LDS, stream>>>(p);
  k_gemm2<true><<<256, 384, G2_LDS, stream>>>(p, 0, 0);
  k_gemm1<false><<<256, 512, G1_LDS, stream>>>(p);
  k_gemm2<false><<<256 + NG_UP + NG_DOWN + NG_FIX, 384, G2_LDS, stream>>>(p, 0, 0);
  k_final<<<128 + NG_UP + NG_DOWN, 384, FE_LDS, stream>>>(p, 0, 0);
}
```

```cpp
#include <hip/hip_runtime.h>
#include <hip/hip_bf16.h>
#include <type_traits>

typedef unsigned short u16;
typedef __attribute__((address_space(3))) void* lds_ptr_t;
using bf16x8 = __attribute__((ext_vector_type(8))) __bf16;
using bf16x2 = __attribute__((ext_vector_type(2))) __bf16;
using f32x4 = __attribute__((ext_vector_type(4))) float;
using f32x2 = __attribute__((ext_vector_type(2))) float;
using u32x2 = __attribute__((ext_vector_type(2))) unsigned;
using u32x4 = __attribute__((ext_vector_type(4))) unsigned;

#if defined(__HIP_DEVICE_COMPILE__)
#pragma clang attribute push(__attribute__((target("no-packed-fp32-ops"))), apply_to = function)
#endif

struct Params {
  const u16 *x0, *Cr, *Ci, *Hr, *Hi, *wr, *wg, *wu, *wd;
  u16* out;
  char* ws;
};

constexpr int SZ_AB = 2 * 8 * 64 * 5 * 1024;
constexpr int SZ_X = 2048 * 640 * 2;
constexpr int WS_HO = 0;
constexpr int HO_H2 = 0;
constexpr int HO_H4 = 27648;
constexpr int HO_KEY3 = 55296;
constexpr int HO_XR3 = 63488;
constexpr int HO_YN = 73728;
constexpr int HO_YB = 83968;
constexpr int HO_KS3 = 94208;
constexpr int HO_BYTES = 98304;
constexpr int WS_T = WS_HO + 131072;
constexpr int WS_CS = WS_T + 40960;
constexpr int WS_ST = WS_CS + 65536;
constexpr int WS_KEY1 = WS_ST + 32768;
constexpr int WS_HSUM = WS_KEY1 + 8192 + 2048;
constexpr int WS_STE = WS_HSUM + 6144;
constexpr int WS_X1 = WS_STE + 16384;
constexpr int WS_AB1 = WS_X1 + SZ_X;
constexpr int WS_AB3 = WS_AB1 + SZ_AB;
constexpr int WS_P1 = WS_AB3 + SZ_AB;
constexpr int WS_P3 = WS_P1 + 4 * SZ_X;
constexpr int WS_WU = WS_P3 + 4 * SZ_X;
constexpr int WS_WD = WS_WU + 108 * 40960;
constexpr int WS_END = WS_WD + 40 * 55296;

#define STAMP_DECL
#define STAMP0(k, i)
#define STAMP_FLUSH(k, wv)
#define P_AUX 0
#define W_AUX 0
#define FIX_WAIT 14
#define FIX_TWO false
#define AB_AUX 0
#define PREF_A 1
#define PREF_C 0
#define PREF_N 8
#define PREF_POL ""
constexpr float EPS = 1e-5f;
constexpr unsigned SENT = 0xFFFFFFFFu;
constexpr int SPIN_CAP = 1 << 18;

#if defined(__HIP_DEVICE_COMPILE__)
#define DEV __device__ __forceinline__
typedef __amdgpu_buffer_rsrc_t rsrc_t;
DEV unsigned umin(unsigned a, unsigned b) { return a < b ? a : b; }
DEV unsigned umax(unsigned a, unsigned b) { return a > b ? a : b; }
DEV unsigned long long ballot(bool c) { return __builtin_amdgcn_ballot_w64(c); }
DEV int popc(unsigned long long m) { return __builtin_popcountll(m); }
DEV rsrc_t mk(const void* p, int bytes) { return __builtin_amdgcn_make_buffer_rsrc((void*)p, 0, bytes, 0x00020000); }
template <int N> DEV void wait_vm() { asm volatile("s_waitcnt vmcnt(%0)" ::"i"(N) : "memory"); }
DEV void wait_lgkm0() { __builtin_amdgcn_s_waitcnt(0xC07F); }
DEV void bar() { __builtin_amdgcn_s_barrier(); }
DEV void lds_bar() { asm volatile("s_waitcnt lgkmcnt(0)\n\ts_barrier" ::: "memory"); }
DEV unsigned pk2(float a, float b) { f32x2 v = {a, b}; return __builtin_bit_cast(unsigned, __builtin_convertvector(v, bf16x2)); }
DEV float lo(unsigned v) { return __builtin_bit_cast(float, v << 16); }
DEV float hi(unsigned v) { return __builtin_bit_cast(float, v & 0xffff0000u); }
DEV float rb(float a) { return lo(pk2(a, a)); }
DEV float dot2(unsigned a, unsigned b, float c) { return __builtin_amdgcn_fdot2_f32_bf16(__builtin_bit_cast(bf16x2, a), __builtin_bit_cast(bf16x2, b), c, false); }
DEV f32x4 mfma(u32x4 a, u32x4 b, f32x4 c) { return __builtin_amdgcn_mfma_f32_16x16x32_bf16(__builtin_bit_cast(bf16x8, a), __builtin_bit_cast(bf16x8, b), c, 0, 0, 0); }
DEV u32x4 ld16(rsrc_t r, int v, int s) { return __builtin_amdgcn_raw_buffer_load_b128(r, v, s, 0); }
DEV u32x4 ld16c(rsrc_t r, int v, int s) { return __builtin_amdgcn_raw_buffer_load_b128(r, v, s, 16); }
DEV unsigned ld4(rsrc_t r, int v, int s) { return __builtin_amdgcn_raw_buffer_load_b32(r, v, s, 0); }
DEV unsigned ld4c(rsrc_t r, int v, int s) { return __builtin_amdgcn_raw_buffer_load_b32(r, v, s, 16); }
DEV void glds(rsrc_t r, char* l, int v, int s) { __builtin_amdgcn_raw_ptr_buffer_load_lds(r, (lds_ptr_t)l, 16, v, s, 0, 0); }
DEV float xq_sum(float x) {
  auto s = __builtin_amdgcn_permlane16_swap(__builtin_bit_cast(unsigned, x), __builtin_bit_cast(unsigned, x), false, false);
  x = __builtin_bit_cast(float, (unsigned)s[0]) + __builtin_bit_cast(float, (unsigned)s[1]);
  auto t = __builtin_amdgcn_permlane32_swap(__builtin_bit_cast(unsigned, x), __builtin_bit_cast(unsigned, x), false, false);
  return __builtin_bit_cast(float, (unsigned)t[0]) + __builtin_bit_cast(float, (unsigned)t[1]);
}
DEV u32x4 selector(int lane, int h) {
  const int j = (lane & 3) + 4 * h;
  const unsigned one = ((lane >> 2) & 3) == (lane >> 4) ? ((j & 1) ? 0x3f800000u : 0x3f80u) : 0u;
  return u32x4{(j >> 1) == 0 ? one : 0u, (j >> 1) == 1 ? one : 0u, (j >> 1) == 2 ? one : 0u, (j >> 1) == 3 ? one : 0u};
}
DEV float diag(const f32x4& g, int fr) { return (fr & 2) ? ((fr & 1) ? g[3] : g[2]) : ((fr & 1) ? g[1] : g[0]); }
DEV unsigned nosent(unsigned v) { return umin(v, 0xFFFFFFFEu); }
DEV bool has_sent(u32x4 v) { return v[0] == SENT || v[1] == SENT || v[2] == SENT || v[3] == SENT; }
DEV unsigned mkkey(float r, int t) {
  unsigned b = pk2(r, r) & 0xffffu;
  b = (b & 0x8000u) ? (~b & 0xffffu) : (b | 0x8000u);
  return (b << 16) | (unsigned)(2047 - t);
}
DEV int key_row(unsigned k) { return 2047 - (int)(k & 0x7ffu); }
DEV float key_val(unsigned k) { unsigned b = k >> 16; b = (b & 0x8000u) ? (b & 0x7fffu) : (~b & 0xffffu); return __builtin_bit_cast(float, b << 16); }
DEV float sigm(float x) { return __builtin_amdgcn_rcpf(1.f + __builtin_amdgcn_exp2f(x * -1.44269504f)); }
template <int C> DEV unsigned dpp_max(unsigned m) { return umax(m, (unsigned)__builtin_amdgcn_mov_dpp((int)m, C, 0xf, 0xf, true)); }

DEV void yield_vm() { __builtin_amdgcn_sched_barrier(0); while (__builtin_amdgcn_s_getreg((31 << 11) | 7) & 0x00C0000Fu) bar(); __builtin_amdgcn_sched_barrier(0); }
DEV void yield_flag(const unsigned* f, unsigned want) {
#pragma unroll 1
  for (int sp = 0; sp < SPIN_CAP; ++sp) {
    if (__builtin_amdgcn_readfirstlane(__hip_atomic_load(f, __ATOMIC_RELAXED, __HIP_MEMORY_SCOPE_WORKGROUP)) >= want) break;
    bar();
  }
  asm volatile("" ::: "memory");
}
DEV void raise_flag(unsigned* f, int lane) {
  asm volatile("s_waitcnt lgkmcnt(0)" ::: "memory");
  if (lane == 0) __hip_atomic_fetch_add(f, 1u, __ATOMIC_RELAXED, __HIP_MEMORY_SCOPE_WORKGROUP);
}

DEV void yield() { __builtin_amdgcn_sched_barrier(0); bar(); __builtin_amdgcn_sched_barrier(0); }
DEV unsigned wave_max(unsigned m) {
  m = dpp_max<0xB1>(m); m = dpp_max<0x4E>(m); m = dpp_max<0x141>(m); m = dpp_max<0x140>(m);
  return umax(umax(__builtin_amdgcn_readlane(m, 0), __builtin_amdgcn_readlane(m, 16)), umax(__builtin_amdgcn_readlane(m, 32), __builtin_amdgcn_readlane(m, 48)));
}

DEV unsigned top8(const char* keys, int lane) {
  const u32x4 ta = *(const u32x4*)(keys + 8192 + lane * 32), tb = *(const u32x4*)(keys + 8192 + lane * 32 + 16);
  yield_vm();
  unsigned m0 = umax(ta[0], tb[3]), m1 = umax(ta[1], tb[2]), m2 = umax(ta[2], tb[1]), m3 = umax(ta[3], tb[0]), res = 0;
  { const unsigned x = umax(m0, m2), y = umin(m0, m2), z = umax(m1, m3), u = umin(m1, m3); m0 = umax(x, z); m1 = umin(x, z); m2 = umax(y, u); m3 = umin(y, u); }
#pragma unroll
  for (int r = 0; r < 8; ++r) {
    const unsigned wv = wave_max(m0);
    res = (lane & 7) == r ? wv : res;
    const bool me = m0 == wv;
    if (r >= 3 && __builtin_expect(ballot(me && m1 == 0) != 0, 0)) {
      unsigned n = 0;
#pragma unroll 1
      for (int i = 0; i < 8; ++i) {
        const u32x4 k = *(const u32x4*)(keys + lane * 128 + i * 16);
#pragma unroll
        for (int e = 0; e < 4; ++e) n = umax(n, k[e] < wv ? k[e] : 0u);
      }
      m1 = me ? n : m1;
    }
    m0 = me ? m1 : m0; m1 = me ? m2 : m1; m2 = me ? m3 : m2; m3 = me ? 0u : m3;
    if (r == 2 || r == 4 || r == 6) yield();
  }
  return res;
}

template <int N> DEV void poll16(u32x4 (&v)[N], rsrc_t r, int voff, int soff, int step) {
#pragma unroll 1
  for (int sp = 0; sp < SPIN_CAP; ++sp) {
    unsigned m = 0;
#pragma unroll
    for (int i = 0; i < N; ++i) v[i] = ld16c(r, voff, soff + i * step);
#pragma unroll
    for (int i = 0; i < N; ++i) m = umax(umax(umax(m, v[i][0]), v[i][1]), umax(v[i][2], v[i][3]));
    if (!ballot(m == SENT)) break;
    __builtin_amdgcn_s_sleep(1);
    asm volatile("" ::: "memory");
  }
}

template <int N, bool TWO> DEV void yield_poll16(u32x4 (&v)[N], rsrc_t r, int vword, int voff, int soff, int step) {
#pragma unroll 1
  for (int sp = 0; TWO && sp < SPIN_CAP; ++sp) {
    const unsigned x = ld4c(r, vword, soff);
    yield_vm();
    if (!ballot(x == SENT)) break;
    bar();
  }
#pragma unroll 1
  for (int sp = 0; sp < SPIN_CAP; ++sp) {
#pragma unroll
    for (int i = 0; i < N; ++i) v[i] = ld16c(r, voff, soff + i * step);
    yield_vm();
    unsigned m = 0;
#pragma unroll
    for (int i = 0; i < N; ++i) m = umax(umax(umax(m, v[i][0]), v[i][1]), umax(v[i][2], v[i][3]));
    if (!ballot(m == SENT)) break;
    bar();
  }
}
#endif

constexpr int G1_STAGE = 28672;
constexpr int G1_ST = 3 * G1_STAGE;
constexpr int G1_HS = G1_ST + 512;
constexpr int G1_W = G1_HS + 640 + 128;
constexpr int G1_LDS = G1_W + 2048;

template <bool FIRST>
__global__ __launch_bounds__(640) void k_gemm1(Params p) {
#if defined(__HIP_DEVICE_COMPILE__)
  extern __shared__ __attribute__((aligned(16))) char smem[];
  const int tid = threadIdx.x, lane = tid & 63;
  const int w = __builtin_amdgcn_readfirstlane(tid >> 6);
  const int b = blockIdx.x, xcd = b & 7, j = b >> 3;
  const int half = xcd & 1, kq = (xcd >> 1) & 1, xm2 = xcd >> 2;
  const int mt = (kq * 2 + xm2) * 8 + (j & 7), ntl = j >> 3;
  const int fr = lane & 15, fq = lane >> 4;
  const int offS = (fr >> 3) * 1024 + (fr & 7) * 128 + ((fq ^ (fr & 7)) << 4);
  STAMP_DECL;
  if (w >= 8) {
    const int lw = w - 8;
    rsrc_t rH = mk((half ? p.Hi : p.Hr) + ntl * 160 * 640, 160 * 1280);
    rsrc_t rX = mk(p.x0 + mt * 64 * 640, 64 * 1280);
    const int r7 = lane >> 3;
    const int voff = (lw * 8 + r7) * 1280 + (((lane & 7) ^ r7) << 4);
    constexpr int PS = FIRST ? 14 : 10;
    auto issue = [&](int g, int slot) {
      char* base = smem + slot * G1_STAGE + lw * 1024;
      if (FIRST) {
#pragma unroll
        for (int i = 0; i < 4; ++i) glds(rX, base + 20480 + i * 2048, voff, g * 128 + i * 16 * 1280);
      }
#pragma unroll
      for (int i = 0; i < 10; ++i) glds(rH, base + i * 2048, voff, g * 128 + i * 16 * 1280);
    };
    if (!FIRST && (lw == 0 || lane < 16)) glds(mk(p.wr, 1280), smem + G1_W + lw * 1024, lw * 1024 + lane * 16, 0);
    issue(0, 0);
    if (FIRST) { issue(1, 1); wait_vm<PS>(); bar(); }
    else { wait_vm<0>(); bar(); issue(1, 1); }
    issue(2, 2);
    int slot = 0;
#pragma unroll 1
    for (int g = 1; g < 8; ++g) {
      wait_vm<PS>(); bar(); issue(g + 2, slot);
      slot = slot == 2 ? 0 : slot + 1;
    }
    wait_vm<PS>(); bar();
    wait_vm<0>(); bar();
    bar();
  } else if (w < 4) {
    STAMP0(FIRST ? 0 : 2, 0);
    const int wm = w & 1, wn = w >> 1;
    float hs[5];
    if (!FIRST) {
#pragma unroll
      for (int n = 0; n < 5; ++n) hs[n] = *(const float*)(p.ws + WS_HSUM + (half * 640 + ntl * 160 + wn * 80 + n * 16 + fr) * 4);
    }
    f32x4 acc[2][5] = {};
    u32x4 ha[2][5], xa[2][2], hb[2][5], xb[2][2];
    const int offX = FIRST ? 20480 + wm * 4096 + offS : 20480 + wm * 2048 + lane * 16;
    auto rd = [&](u32x4 (&x)[2][2], u32x4 (&h)[2][5], int slot) {
      const char* base = smem + slot * G1_STAGE;
#pragma unroll
      for (int par = 0; par < 2; ++par) {
        const char* q = base + wn * 10240 + (offS ^ (par << 6));
        const char* px = base + (FIRST ? (offX ^ (par << 6)) : offX + par * 4096);
#pragma unroll
        for (int i = 0; i < 2; ++i) x[par][i] = *(const u32x4*)(px + i * (FIRST ? 2048 : 1024));
#pragma unroll
        for (int n = 0; n < 5; ++n) h[par][n] = *(const u32x4*)(q + n * 2048);
      }
    };
    auto mm = [&](u32x4 (&x)[2][2], u32x4 (&h)[2][5]) {
#pragma unroll
      for (int par = 0; par < 2; ++par)
#pragma unroll
        for (int n = 0; n < 5; ++n)
#pragma unroll
          for (int i = 0; i < 2; ++i) acc[i][n] = mfma(x[par][i], h[par][n], acc[i][n]);
    };
    auto sched = [&]() {
#pragma unroll
      for (int q = 0; q < 14; ++q) { __builtin_amdgcn_sched_group_barrier(0x8, 1, 0); __builtin_amdgcn_sched_group_barrier(0x100, 1, 0); }
      __builtin_amdgcn_sched_group_barrier(0x8, 6, 0);
    };
    rsrc_t rC = mk((half ? p.Ci : p.Cr) + (xm2 * 1024 + j * 32) * 2048 + kq * 1024, 32 * 4096);
    unsigned sink = 0;
    auto touch = [&](int i) {
      if ((FIRST ? PREF_A : PREF_C) && (i >> 1) == w) asm volatile("buffer_load_dword %0, %1, %2, %3 offen " PREF_POL : "+v"(sink) : "v"((lane >> 4) * 4096 + (lane & 15) * 128), "s"(rC), "s"(i * 4 * 4096) : "memory");
    };
#pragma unroll
    for (int i = 0; i < 2; ++i)
#pragma unroll
      for (int n = 0; n < 5; ++n) asm volatile("" : "+v"(acc[i][n]));
    bar();
    STAMP0(FIRST ? 0 : 2, 1);
    rd(xa, ha, 0);
    int slot = 1;
#pragma unroll 1
    for (int g = 0; g < 8; g += 2) {
      wait_lgkm0(); bar();
      __builtin_amdgcn_sched_barrier(0);
      rd(xb, hb, slot); mm(xa, ha); sched();
      __builtin_amdgcn_sched_barrier(0);
      touch(g);
      slot = slot == 2 ? 0 : slot + 1;
      wait_lgkm0(); bar();
      __builtin_amdgcn_sched_barrier(0);
      rd(xa, ha, slot); mm(xb, hb); sched();
      __builtin_amdgcn_sched_barrier(0);
      touch(g + 1);
      slot = slot == 2 ? 0 : slot + 1;
    }
    wait_lgkm0(); bar();
    __builtin_amdgcn_sched_barrier(0);
    rd(xb, hb, 0); mm(xa, ha); sched();
    __builtin_amdgcn_sched_barrier(0);
    mm(xb, hb);
    STAMP0(FIRST ? 0 : 2, 2);
    bar();
    if (FIRST) {
#pragma unroll
      for (int n = 0; n < 5; ++n) hs[n] = *(const float*)(smem + G1_HS + (wn * 80 + n * 16 + fr) * 4);
    }
    rsrc_t rO = mk(p.ws + (FIRST ? WS_AB1 : WS_AB3) + ((half * 8 + ntl * 2 + wn) * 64 + mt * 2 + wm) * 5120, 5120);
#pragma unroll
    for (int i = 0; i < 2; ++i) {
      const f32x4 mi = *(const f32x4*)(smem + G1_ST + (wm * 32 + i * 16 + fq * 4) * 4), si = *(const f32x4*)(smem + G1_ST + 256 + (wm * 32 + i * 16 + fq * 4) * 4);
#pragma unroll
      for (int n = 0; n < 5; ++n) {
        const f32x4 v = si * (acc[i][n] - mi * hs[n]);
        __builtin_amdgcn_raw_buffer_store_b64(u32x2{pk2(v[0], v[1]), pk2(v[2], v[3])}, rO, (fq >> 1) * 256 + fr * 16 + (fq & 1) * 8 + n * 1024 + i * 512, 0, AB_AUX);
      }
    }
    STAMP0(FIRST ? 0 : 2, 3);
    STAMP_FLUSH(FIRST ? 0 : 2, 0);
    asm volatile("s_waitcnt vmcnt(0)" : "+v"(sink)::"memory");
  } else {
    const int lw = w - 4;
    const int t16 = mt * 4 + lw, s0 = t16 * 16;
    f32x4 sum = {}, sq = {};
    __builtin_amdgcn_s_setprio(2);
    if (FIRST) {
      if (lw == 0 && lane < 24) *(u32x4*)(p.ws + WS_HO + (b * 24 + lane) * 16) = u32x4{SENT, SENT, SENT, SENT};
      f32x4 hs[3] = {};
      const u32x4 ones = {0x3f803f80u, 0x3f803f80u, 0x3f803f80u, 0x3f803f80u};
      int slot = 0;
#pragma unroll 1
      for (int g = 0; g < 10; ++g) {
        bar();
        const char* base = smem + slot * G1_STAGE + lw * 2048;
        slot = slot == 2 ? 0 : slot + 1;
#pragma unroll
        for (int par = 0; par < 2; ++par) {
          const char* q = base + (offS ^ (par << 6));
          const u32x4 x = *(const u32x4*)(q + 20480);
          sum = mfma(ones, x, sum);
          sq = mfma(x, x, sq);
          hs[0] = mfma(ones, *(const u32x4*)q, hs[0]);
          hs[1] = mfma(ones, *(const u32x4*)(q + 8192), hs[1]);
          hs[2] = mfma(ones, *(const u32x4*)(q + (lw < 2 ? 16384 : 0)), hs[2]);
        }
        wait_lgkm0();
      }
      if (fq == (fr >> 2)) {
        const float mean = sum[0] * (1.f / 640.f);
        const float var = __builtin_fmaxf(diag(sq, fr) * (1.f / 640.f) - mean * mean, 0.f);
        *(float*)(smem + G1_ST + (lw * 16 + fr) * 4) = mean; *(float*)(smem + G1_ST + 256 + (lw * 16 + fr) * 4) = __builtin_amdgcn_rsqf(var + EPS);
        float* h = (float*)(smem + G1_HS);
        h[lw * 16 + fr] = hs[0][0];
        h[(lw + 4) * 16 + fr] = hs[1][0];
        if (lw < 2) h[(lw + 8) * 16 + fr] = hs[2][0];
        if (mt == 0) {
          float* g = (float*)(p.ws + WS_HSUM) + half * 640 + ntl * 160;
          g[lw * 16 + fr] = hs[0][0];
          g[(lw + 4) * 16 + fr] = hs[1][0];
          if (lw < 2) g[(lw + 8) * 16 + fr] = hs[2][0];
        }
      }
      lds_bar();
    } else {
      const int u = half * 4 + ntl;
      rsrc_t rP = mk(p.ws + WS_P1 + t16 * 20480, 4 * SZ_X - t16 * 20480);
      rsrc_t rO = mk(p.ws + WS_X1 + t16 * 20480, 20480);
      u32x4 raw[2][4];
#pragma unroll
      for (int c = 0; c < 2; ++c)
#pragma unroll
        for (int a = 0; a < 4; ++a) raw[c][a] = ld16(rP, lane * 16, a * SZ_X + c * 1024);
      int slot = 0;
      const u32x4 sel0 = selector(lane, 0), sel1 = selector(lane, 1);
      const u32x4 ones = {0x3f803f80u, 0x3f803f80u, 0x3f803f80u, 0x3f803f80u};
      f32x4 sw = {};
      auto chunk = [&](u32x4 (&r)[4], int c, int par, bool more) {
        f32x4 v0 = {}, v1 = {};
#pragma unroll
        for (int a = 0; a < 4; ++a) {
          v0 = mfma(sel0, r[a], v0); v1 = mfma(sel1, r[a], v1);
          __builtin_amdgcn_sched_barrier(0);
          if (more) r[a] = ld16(rP, lane * 16, a * SZ_X + (c + 2) * 1024);
          __builtin_amdgcn_sched_barrier(0);
        }
        const u32x4 o = {pk2(v0[0], v0[1]), pk2(v0[2], v0[3]), pk2(v1[0], v1[1]), pk2(v1[2], v1[3])};
        *(u32x4*)(smem + slot * G1_STAGE + 20480 + par * 4096 + lw * 1024 + lane * 16) = o;
        if ((c & 7) == u) __builtin_amdgcn_raw_buffer_store_b128(o, rO, lane * 16, c * 1024, 0);
        return o;
      };
      auto stats = [&](const u32x4& o, int c) {
        u32x4 a = *(const u32x4*)(smem + G1_W + c * 64 + fq * 16);
        if (!(fr & 1)) a = ones;
        sum = mfma(a, o, sum);
        sq = mfma(o, o, sq);
        sw = mfma(a, ones, sw);
      };
      auto stage = [&](int g, bool more) {
        const u32x4 o0 = chunk(raw[0], 2 * g, 0, more), o1 = chunk(raw[1], 2 * g + 1, 1, more);
        lds_bar();
        stats(o0, 2 * g); stats(o1, 2 * g + 1);
        slot = slot == 2 ? 0 : slot + 1;
      };
#pragma unroll 1
      for (int g = 0; g < 9; ++g) stage(g, true);
      stage(9, false);
      unsigned key = 0;
      if (fq == (fr >> 2)) {
        const float mean = sum[0] * (1.f / 640.f);
        const float var = __builtin_fmaxf(diag(sq, fr) * (1.f / 640.f) - mean * mean, 0.f);
        const float g1 = 1.f + __builtin_amdgcn_rsqf(var + EPS);
        const float scale = g1 * __builtin_amdgcn_rsqf(g1 * g1 * var + EPS);
        *(float*)(smem + G1_ST + (lw * 16 + fr) * 4) = mean; *(float*)(smem + G1_ST + 256 + (lw * 16 + fr) * 4) = scale;
        if (u == 0) {
          const float mb = rb(mean), rs = __builtin_amdgcn_rcpf(rb(__builtin_amdgcn_sqrtf(rb(rb(var) + EPS))));
          *(f32x4*)(p.ws + WS_ST + (s0 + fr) * 16) = f32x4{mb, rs, mean, scale};
          *(f32x2*)(p.ws + WS_STE + (s0 + fr) * 8) = f32x2{1.f + rs, -mb * rs};
          key = mkkey((sum[1] - mb * sw[1]) * rs, s0 + fr);
          *(unsigned*)(p.ws + WS_KEY1 + (s0 + fr) * 4) = key;
          if (s0 + fr == 0) *(float*)(p.ws + WS_HSUM + 5120) = sw[1];
        }
      }
      lds_bar();
      if (u == 0) {
        u32x4 m;
#pragma unroll
        for (int i = 0; i < 4; ++i) { m[i] = wave_max(key); key = key == m[i] ? 0u : key; }
        if (lane == 0) *(u32x4*)(p.ws + WS_KEY1 + 8192 + t16 * 16) = m;
      }
    }
  }
#endif
}

#if defined(__HIP_DEVICE_COMPILE__)
constexpr int FL_FLAG = 0;
constexpr int FL_TOP = 64;
constexpr int FL_RED = 256;
constexpr int FL_H = FL_RED + 12288;
constexpr int FL_END = FL_H + 20480;

DEV unsigned swiglu2(const float* red, int nw, int tok, int cp, float mb, float rs) {
  float v[2][2];
#pragma unroll
  for (int m = 0; m < 2; ++m)
#pragma unroll
    for (int c = 0; c < 2; ++c) {
      float a = 0.f, o = 0.f;
#pragma unroll
      for (int q = 0; q < nw; ++q) {
        a += red[(q * 128 + m * 64 + (tok >> 2) * 16 + cp * 2 + c) * 4 + (tok & 3)];
        o += red[(q * 128 + m * 64 + 32 + cp * 2 + c) * 4];
      }
      v[m][c] = rb((a - mb * o) * rs);
    }
  return nosent(pk2(rb(v[0][0] * sigm(v[0][0])) * v[1][0], rb(v[0][1] * sigm(v[0][1])) * v[1][1]));
}

DEV void ffn4_up(const Params& p, char* smem, int g) {
  const int tid = threadIdx.x, lane = tid & 63, fr = lane & 15, fq = lane >> 4;
  const int w = __builtin_amdgcn_readfirstlane(tid >> 6);
  if (w > 5) return;
  rsrc_t rHO = mk(p.ws + WS_HO, HO_BYTES);
  float* red = (float*)(smem + FL_RED);
  STAMP_DECL;
  STAMP0(6, 0);
  if (w < 5) {
    u32x4 f[4][2], xa[4];
    rsrc_t rw = mk(p.ws + WS_WU + g * 40960, 40960);
#pragma unroll
    for (int i = 0; i < 4; ++i) { f[i][0] = ld16(rw, lane * 16, (w * 4 + i) * 1024); f[i][1] = ld16(rw, lane * 16, (20 + w * 4 + i) * 1024); }
    poll16<4>(xa, rHO, fq * 128 + (fr & 7) * 16, HO_XR3 + w * 2048, 512);
    STAMP0(6, 2);
    f32x4 a0 = {}, a1 = {};
#pragma unroll
    for (int i = 0; i < 4; ++i) {
#pragma unroll
      for (int e = 0; e < 4; ++e) xa[i][e] = fr < 8 ? xa[i][e] : 0x3f803f80u;
      a0 = mfma(xa[i], f[i][0], a0); a1 = mfma(xa[i], f[i][1], a1);
    }
    *(f32x4*)(red + (w * 128 + lane) * 4) = a0; *(f32x4*)(red + (w * 128 + 64 + lane) * 4) = a1;
    lds_bar();
    STAMP0(6, 3);
    STAMP_FLUSH(6, 0);
  } else {
    const int tok = lane >> 3, cp = lane & 7;
    u32x4 ks[1];
    poll16<1>(ks, rHO, tok * 16, HO_KS3, 0);
    bar();
    const unsigned h = swiglu2(red, 5, tok, cp, __builtin_bit_cast(float, (unsigned)ks[0][1]), __builtin_bit_cast(float, (unsigned)ks[0][2]));
    __builtin_amdgcn_raw_buffer_store_b32(h, rHO, ((g & 1) * 2 + (cp >> 2)) * 128 + tok * 16 + (cp & 3) * 4, HO_H4 + (g >> 1) * 512, 16);
  }
}

DEV void ffn4_down(const Params& p, char* smem, int g) {
  const int tid = threadIdx.x, lane = tid & 63, fr = lane & 15, fq = lane >> 4;
  const int w = __builtin_amdgcn_readfirstlane(tid >> 6);
  if (w > 6) return;
  rsrc_t rHO = mk(p.ws + WS_HO, HO_BYTES);
  float* red = (float*)(smem + FL_RED);
  STAMP_DECL;
  if (w < 6) {
    u32x4 f[9], ha[9];
    rsrc_t rw = mk(p.ws + WS_WD + g * 55296, 55296);
#pragma unroll
    for (int i = 0; i < 9; ++i) f[i] = ld16(rw, lane * 16, (w * 9 + i) * 1024);
    bar();
    poll16<9>(ha, rHO, fq * 128 + (fr & 7) * 16, HO_H4 + w * 9 * 512, 512);
    STAMP0(6, 4);
    f32x4 a0 = {}, a1 = {};
#pragma unroll
    for (int i = 0; i < 9; ++i) { if (i & 1) a1 = mfma(ha[i], f[i], a1); else a0 = mfma(ha[i], f[i], a0); }
    if (fq < 2) *(f32x4*)(red + (w * 32 + lane) * 4) = a0 + a1;
    lds_bar();
    STAMP0(6, 5);
    STAMP_FLUSH(6, 0);
  } else {
    const int tok = lane >> 3, cp = lane & 7;
    u32x4 ks;
    unsigned xr;
    for (int sp = 0; sp < SPIN_CAP; ++sp) {
      ks = ld16c(rHO, tok * 16, HO_KS3); xr = ld4c(rHO, (g >> 1) * 512 + ((g & 1) * 2 + (cp >> 2)) * 128 + tok * 16 + (cp & 3) * 4, HO_XR3);
      if (!ballot(has_sent(ks) || xr == SENT)) break;
      __builtin_amdgcn_s_sleep(2);
      asm volatile("" ::: "memory");
    }
    bar();
    const float mb = __builtin_bit_cast(float, (unsigned)ks[1]), rs = __builtin_bit_cast(float, (unsigned)ks[2]);
    const float sg = rb(sigm(key_val(ks[0])));
    const float n0 = rb(rb(lo(xr) - mb) * rs), n1 = rb(rb(hi(xr) - mb) * rs);
    bar();
    const int o = ((tok >> 2) * 16 + cp * 2) * 4 + (tok & 3);
    float y0 = 0.f, y1 = 0.f;
#pragma unroll
    for (int q = 0; q < 6; ++q) { y0 += red[q * 128 + o]; y1 += red[q * 128 + 4 + o]; }
    *(unsigned*)((char*)p.out + key_row(ks[0]) * 1280 + g * 32 + cp * 4) = pk2(lo(xr) + rb(n0 + rb(rb(y0) * sg)), hi(xr) + rb(n1 + rb(rb(y1) * sg)));
  }
}

DEV unsigned ffn2_front(const Params& p, char* sm, int a, int lane) {
  if (a == 0 && lane < 4) ((unsigned*)(sm + FL_FLAG))[lane] = 0;
  asm volatile("s_waitcnt lgkmcnt(0)" ::: "memory");
  return top8(p.ws + WS_KEY1, lane);
}

DEV void ffn2_up(const Params& p, char* sm, int g, int a, int lane) {
  const int fr = lane & 15, fq = lane >> 4, tok = lane >> 3, cp = lane & 7;
  unsigned* fl = (unsigned*)(sm + FL_FLAG);
  float* red = (float*)(sm + FL_RED);
  STAMP_DECL;
  STAMP0(4, 0);
  rsrc_t rw = mk(p.ws + WS_WU + g * 40960 + a * 5120, 40960), rX = mk(p.ws + WS_X1 + a * 5120, SZ_X - a * 5120);
  const int r = (a - 1) * 8 + tok;
  const unsigned key = ffn2_front(p, sm, a, lane);
  STAMP0(4, 1);
  u32x4 f[5][2], xa[5];
  const int row = key_row(key);
  const int vx = (row >> 4) * 20480 + fq * 256 + (row & 15) * 16;
#pragma unroll
  for (int i = 0; i < 5; ++i) xa[i] = u32x4{0x3f803f80u, 0x3f803f80u, 0x3f803f80u, 0x3f803f80u};
  f32x2 st = {};
#pragma unroll
  for (int i = 0; i < 5; ++i) {
    yield();
    if (fr < 8) xa[i] = ld16(rX, vx, i * 1024);
    f[i][0] = ld16(rw, lane * 16, i * 1024); f[i][1] = ld16(rw, lane * 16, 20480 + i * 1024);
  }
  if (a == 0) st = *(const f32x2*)(p.ws + WS_ST + __builtin_amdgcn_ds_bpermute(tok * 4, row) * 16);
  yield_vm();
  STAMP0(4, 2);
  f32x4 a0 = {}, a1 = {};
#pragma unroll
  for (int i = 0; i < 5; ++i) { a0 = mfma(xa[i], f[i][0], a0); a1 = mfma(xa[i], f[i][1], a1); }
  *(f32x4*)(red + (a * 128 + lane) * 4) = a0; *(f32x4*)(red + (a * 128 + 64 + lane) * 4) = a1;
  raise_flag(fl + 1, lane);
  if (a) {
    yield();
    typedef const __attribute__((address_space(1))) u16* gp;
    const gp C = (gp)(cp < 4 ? p.Cr : p.Ci) + umin(g * 19 + r, 2047) * 2048;
    const int c0 = __builtin_amdgcn_ds_bpermute((cp & 3) * 8, row), c1 = __builtin_amdgcn_ds_bpermute((cp & 3) * 8 + 4, row);
    yield();
    const unsigned v0 = C[c0], v1 = C[c1];
    yield_vm();
    if (r < 19 && g * 19 + r < 2048) *(unsigned*)(p.ws + WS_CS + (g * 19 + r) * 32 + cp * 4) = v0 | (v1 << 16);
    return;
  }
  yield();
  yield_flag(fl + 1, 4);
  float v[2][2], x[2][2][4], o[2][2][4];
#pragma unroll
  for (int m = 0; m < 2; ++m)
#pragma unroll
    for (int c = 0; c < 2; ++c)
#pragma unroll
      for (int q = 0; q < 4; ++q) {
        x[m][c][q] = red[(q * 128 + m * 64 + (tok >> 2) * 16 + cp * 2 + c) * 4 + (tok & 3)];
        o[m][c][q] = red[(q * 128 + m * 64 + 32 + cp * 2 + c) * 4];
      }
  yield();
#pragma unroll
  for (int m = 0; m < 2; ++m)
#pragma unroll
    for (int c = 0; c < 2; ++c) v[m][c] = rb(((x[m][c][0] + x[m][c][1]) + (x[m][c][2] + x[m][c][3]) - st[0] * ((o[m][c][0] + o[m][c][1]) + (o[m][c][2] + o[m][c][3]))) * st[1]);
  yield();
  const unsigned h = nosent(pk2(rb(v[0][0] * sigm(v[0][0])) * v[1][0], rb(v[0][1] * sigm(v[0][1])) * v[1][1]));
  __builtin_amdgcn_raw_buffer_store_b32(h, mk(p.ws + WS_HO, HO_BYTES), ((g & 1) * 2 + (cp >> 2)) * 128 + tok * 16 + (cp & 3) * 4, HO_H2 + (g >> 1) * 512, 16);
  STAMP0(4, 3);
  STAMP_FLUSH(4, 8);
}

DEV void ffn2_down(const Params& p, char* sm, int g, int a, int lane) {
  const int fr = lane & 15, fq = lane >> 4, tok = lane >> 3, cp = lane & 7;
  unsigned* fl = (unsigned*)(sm + FL_FLAG);
  float* red = (float*)(sm + FL_RED);
  rsrc_t rHO = mk(p.ws + WS_HO, HO_BYTES);
  STAMP_DECL;
  const int po = (g >> 1) * 512 + ((g & 1) * 2 + (cp >> 2)) * 128 + tok * 16 + (cp & 3) * 4;
  unsigned key = 0, xr = 0;
  int xo = 0;
  f32x4 st = {};
  if (a == 0) {
    key = __builtin_amdgcn_ds_bpermute(tok * 4, ffn2_front(p, sm, a, lane));
    yield();
    xo = (key_row(key) >> 4) * 20480 + (po >> 7) * 256 + (key_row(key) & 15) * 16 + (cp & 3) * 4;
    xr = *(const unsigned*)(p.ws + WS_X1 + xo);
    st = *(const f32x4*)(p.ws + WS_ST + key_row(key) * 16);
  } else {
    yield(); yield();
  }
  yield();
  u32x4 f[14], ha[7];
  const int c0 = a == 3 ? 40 : a * 14;
  rsrc_t rw = mk(p.ws + WS_WD + g * 55296 + c0 * 1024, 14336);
#pragma unroll
  for (int i = 0; i < 14; ++i) { f[i] = ld16(rw, lane * 16, i * 1024); yield(); }
  float sg = 0.f, n0 = 0.f, n1 = 0.f;
  if (a == 0) {
    __builtin_amdgcn_raw_buffer_store_b32(nosent(pk2((lo(xr) - st[2]) * st[3], (hi(xr) - st[2]) * st[3])), rHO, po, HO_YB, 16);
    if (g == 0 && cp == 0) *(f32x2*)(p.ws + WS_STE + key_row(key) * 8) = f32x2{1.f, 0.f};
    yield();
    sg = rb(sigm(key_val(key)));
    n0 = rb(rb(lo(xr) - st[0]) * st[1]); n1 = rb(rb(hi(xr) - st[0]) * st[1]);
    asm volatile("" : "+v"(sg), "+v"(n0), "+v"(n1));
    yield();
  }
  yield_poll16<7, true>(ha, rHO, umin(lane, 27) * 256, (fr >> 3) * 512 + fq * 128 + (fr & 7) * 16, HO_H2 + c0 * 512, 1024);
  STAMP0(4, 4);
  yield();
  if (a == 3) { f[0] = u32x4{}; f[1] = u32x4{}; }
  f32x4 a0 = {}, a1 = {};
#pragma unroll
  for (int i = 0; i < 7; ++i) { a0 = mfma(ha[i], f[2 * i], a0); a1 = mfma(ha[i], f[2 * i + 1], a1); }
  *(f32x4*)(red + (a * 64 + lane) * 4) = fq < 2 ? a0 : a1;
  raise_flag(fl + 1, lane);
  if (a) return;
  yield();
  yield_flag(fl + 1, 4);
  const int o = ((tok >> 2) * 16 + cp * 2) * 4 + (tok & 3);
  float y0 = 0.f, y1 = 0.f;
#pragma unroll
  for (int q = 0; q < 8; ++q) { y0 += red[q * 128 + o]; y1 += red[q * 128 + 4 + o]; }
  asm volatile("" : "+v"(y0), "+v"(y1));
  yield();
  const unsigned res = pk2(lo(xr) + rb(n0 + rb(rb(y0) * sg)), hi(xr) + rb(n1 + rb(rb(y1) * sg)));
  __builtin_amdgcn_raw_buffer_store_b32(nosent(res), rHO, po, HO_YN, 16);
  *(unsigned*)(p.ws + WS_X1 + xo) = res;
  STAMP0(4, 5);
  STAMP_FLUSH(4, 8);
}

DEV void ffn2_fix(const Params& p, char* sm, int g, int a, int lane) {
  const int fr = lane & 15, fq = lane >> 4;
  unsigned* fl = (unsigned*)(sm + FL_FLAG);
  float* red = (float*)(sm + FL_RED);
  rsrc_t rHO = mk(p.ws + WS_HO, HO_BYTES);
  const int half = g >= 40, e0 = (g - half * 40) * 16, e = e0 + fr;
  STAMP_DECL;
  if (a == 0 && lane < 4) fl[lane] = 0;
  asm volatile("s_waitcnt lgkmcnt(0)" ::: "memory");
  rsrc_t rH = mk((half ? p.Hi : p.Hr) + e0 * 640 + a * 160, 16 * 1280 - a * 320);
  char* hl = sm + FL_H + a * 5120;
  int to = ((e >> 5) * 2 + ((e >> 2) & 1)) * 1024 + (half * 2 + (fq >> 1)) * 256 + (((e >> 3) & 3) * 4 + (e & 3)) * 16 + (fq & 1) * 8;
  asm volatile("" : "+v"(to));
#pragma unroll 1
  for (int i = 0; i < 4; ++i) bar();
  u32x4 hf[5], xa[5];
#pragma unroll
  for (int i = 0; i < 5; ++i) { glds(rH, hl + i * 1024, (lane >> 2) * 1280 + (((lane & 3) ^ ((lane >> 3) & 3)) << 4), i * 64); yield(); }
  const float hs = *(const float*)(p.ws + WS_HSUM + (half * 640 + e) * 4);
  yield_vm();
  wait_vm<0>();
#pragma unroll
  for (int i = 0; i < 5; ++i) hf[i] = *(const u32x4*)(hl + i * 1024 + fr * 64 + ((fq ^ ((fr >> 1) & 3)) << 4));
#pragma unroll 1
  for (int i = 0; i < FIX_WAIT; ++i) bar();
  yield_poll16<5, FIX_TWO>(xa, rHO, umin(lane, 9) * 256, (fr >> 3) * (HO_YB - HO_YN) + fq * 128 + (fr & 7) * 16, HO_YN + a * 2560, 512);
  STAMP0(4, 6);
  yield();
  const u32x4 ones = {0x3f803f80u, 0x3f803f80u, 0x3f803f80u, 0x3f803f80u};
  f32x4 s = {}, q = {}, acc = {};
#pragma unroll
  for (int i = 0; i < 5; ++i) { acc = mfma(xa[i], hf[i], acc); s = mfma(xa[i], ones, s); q = mfma(xa[i], xa[i], q); }
  *(f32x4*)(red + (a * 64 + lane) * 4) = acc;
  if (fr == 0) *(f32x4*)(red + 1280 + a * 32 + fq * 4) = s;
  if (fq == (fr >> 2)) red[1280 + a * 32 + 16 + fr] = diag(q, fr);
  raise_flag(fl + 1, lane);
  if (a) return;
  yield();
  yield_flag(fl + 1, 4);
  acc = s = q = f32x4{};
#pragma unroll
  for (int k = 0; k < 4; ++k) { acc += *(const f32x4*)(red + (k * 64 + lane) * 4); s += *(const f32x4*)(red + 1280 + k * 32 + fq * 4); q += *(const f32x4*)(red + 1280 + k * 32 + 16 + fq * 4); }
  asm volatile("" : "+v"(acc), "+v"(s), "+v"(q));
  yield();
  if (fq < 2) {
#pragma unroll
    for (int r = 0; r < 4; ++r) {
      const float mean = s[r] * (1.f / 640.f), var = __builtin_fmaxf(q[r] * (1.f / 640.f) - mean * mean, 0.f);
      acc[r] = (acc[r] - mean * hs) * __builtin_amdgcn_rsqf(var + EPS);
    }
  }
  *(u32x2*)(p.ws + WS_T + to) = u32x2{pk2(acc[0], acc[1]), pk2(acc[2], acc[3])};
  STAMP0(4, 7);
  STAMP_FLUSH(4, 8);
}
#endif

constexpr int G2_B = 3 * 16384;
constexpr int G2_SIDE = G2_B + 3 * 10240;
constexpr int G2_LDS = G2_SIDE + 43008 + 16;
constexpr int G2_X0 = G2_SIDE, G2_WT = G2_X0 + 10240;
constexpr int NG_UP = 108, NG_DOWN = 40, NG_FIX = 80;

template <bool FIRST>
__global__ __launch_bounds__(768) void k_gemm2(Params p) {
#if defined(__HIP_DEVICE_COMPILE__)
  static_assert(FL_END <= 43008, "side LDS");
  extern __shared__ __attribute__((aligned(16))) char smem[];
  const int b = blockIdx.x;
  const int tid = threadIdx.x, lane = tid & 63;
  const int w = __builtin_amdgcn_readfirstlane(tid >> 6);
  const int xcd = b & 7, j = b >> 3;
  const int half = xcd & 1, kq = (xcd >> 1) & 1, mt = (xcd >> 2) * 8 + (j & 7), nt = j >> 3;
  const int lw = w & 3;
  if (w >= 8) {
    if (!FIRST) {
      if (b < NG_UP) ffn2_up(p, smem + G2_SIDE, b, lw, lane);
      else if (b < NG_UP + NG_DOWN) ffn2_down(p, smem + G2_SIDE, b - NG_UP, lw, lane);
      else if (b < NG_UP + NG_DOWN + NG_FIX) ffn2_fix(p, smem + G2_SIDE, b - NG_UP - NG_DOWN, lw, lane);
      return;
    }
    const int un = b * 4 + lw;
    char* img = smem + G2_WT + lw * 8192;
    const bool up = un < 540;
    const int cb = up ? un / 20 : (un - 540) / 54, c = up ? un % 20 : (un - 540) % 54;
    const int ld = up ? 3456 : 1280;
    const char* m[2];
#pragma unroll
    for (int i = 0; i < 2; ++i) {
      const int src = (lane & 15) ^ (((lane >> 4) | (i << 2)) << 1);
      m[i] = (up ? (const char*)(src < 8 ? p.wg : p.wu) + cb * 128 + (src & 7) * 16
                 : (const char*)p.wd + cb * 256 + src * 16) + (c * 32 + (lane >> 4)) * ld;
      asm volatile("" : "+v"(m[i]));
    }
    rsrc_t rX = mk(p.x0 + (mt * 128 + (half * 2 + kq) * 16) * 640 + nt * 160, 80 * 1280);
    int vx[3];
#pragma unroll
    for (int i = 0; i < 3; ++i) {
      const int pc = (i * 4 + lw) * 64 + lane, r = pc / 20;
      vx[i] = ((r >> 4) * 64 + (r & 15)) * 1280 + (pc % 20) * 16;
      asm volatile("" : "+v"(vx[i]));
    }
    rsrc_t rd = up ? mk(p.ws + WS_WU + cb * 4 * 40960 + c * 1024, 4 * 40960) : mk(p.ws + WS_WD + cb * 8 * 55296 + c * 1024, 8 * 55296);
    const int rr = (lane >> 2) & 3, q = lane >> 4;
    const unsigned base = (unsigned)(G2_WT + lw * 8192 + (q * 8 + rr) * 256 + ((rr | ((q & 1) << 2)) << 5) + (lane & 3) * 8);
    if (lw == 0 && lane == 0) *(unsigned*)(smem + G2_LDS - 16) = 0;
    asm volatile("s_waitcnt lgkmcnt(0)" ::: "memory");
#pragma unroll 1
    for (int n = 0; n < 4; ++n) bar();
#pragma unroll
    for (int i = 0; i < 8; ++i) {
      if (un < 810) __builtin_amdgcn_global_load_lds((const __attribute__((address_space(1))) void*)(m[(i >> 1) & 1] + i * 4 * ld), (lds_ptr_t)(img + i * 1024), 16, 0, 0);
      yield();
    }
#pragma unroll
    for (int i = 0; i < 3; ++i)
      if (i < 2 || lw < 2) glds(rX, smem + G2_X0 + (i * 4 + lw) * 1024, vx[i], 0);
    yield_vm();
    wait_vm<0>();
    raise_flag((unsigned*)(smem + G2_LDS - 16), lane);
    if (un >= 810) return;
#pragma unroll
    for (int n = 0; n < 8; ++n) {
      u32x2 t0, t1;
      yield();
      asm volatile("ds_read_b64_tr_b16 %0, %2\n\tds_read_b64_tr_b16 %1, %2 offset:1024\n\ts_waitcnt lgkmcnt(0)" : "=&v"(t0), "=&v"(t1) : "v"(base ^ (n << 5)) : "memory");
      __builtin_amdgcn_raw_buffer_store_b128(u32x4{t0[0], t0[1], t1[0], t1[1]}, rd, lane * 16, up ? (n & 3) * 40960 + (n >> 2) * 20480 : n * 55296, W_AUX);
    }
    return;
  }
  if (w >= 4) {
    rsrc_t rA = mk((half ? p.Ci : p.Cr) + mt * 128 * 2048 + kq * 1024, 128 * 4096 - kq * 2048);
    rsrc_t rB = mk(p.ws + (FIRST ? WS_AB1 : WS_AB3) + ((half * 8 + nt * 2) * 64 + kq * 32) * 5120, 96 * 5120);
    const int r7 = lane >> 3;
    const int voffA = (lw * 8 + r7) * 4096 + (((lane & 7) ^ r7) << 4);
    int soffB[3];
#pragma unroll
    for (int i = 0; i < 3; ++i) { const int pc = i * 4 + lw; soffB[i] = pc < 5 ? pc * 1024 : 64 * 5120 + (pc - 5) * 1024; }
    auto issueAh = [&](int t, int slot, int h) {
#pragma unroll
      for (int i = 0; i < 2; ++i) glds(rA, smem + slot * 16384 + h * 8192 + (i * 4 + lw) * 1024, voffA, t * 128 + (i * 32 + h * 64) * 4096);
    };
    auto body = [&](auto pb) {
      constexpr int PB = decltype(pb)::value, PH = 2, W = 4 + PB;
      auto issueB = [&](int n, int slot) {
#pragma unroll
        for (int i = 0; i < PB; ++i) glds(rB, smem + G2_B + slot * 10240 + (i * 4 + lw) * 1024, lane * 16, n * 5120 + soffB[i]);
      };
      issueAh(0, 0, 0); issueAh(0, 0, 1); issueB(0, 0); issueB(1, 1); issueAh(1, 1, 0);
      wait_vm<PB + PH>(); bar(); issueAh(1, 1, 1); issueB(2, 2);
      wait_vm<W>(); bar(); issueB(3, 0); issueAh(2, 2, 0); issueAh(2, 2, 1);
      wait_vm<W>(); bar(); issueB(4, 1); issueAh(3, 0, 0);
      for (int n = 2; n < 26; n += 6) {
        const int t = n >> 1;
        wait_vm<W>(); bar(); issueB(n + 3, 2); issueAh(t + 2, 0, 1);
        wait_vm<W>(); bar(); issueB(n + 4, 0); issueAh(t + 3, 1, 0);
        wait_vm<W>(); bar(); issueB(n + 5, 1); issueAh(t + 3, 1, 1);
        wait_vm<W>(); bar(); issueB(n + 6, 2); issueAh(t + 4, 2, 0);
        wait_vm<W>(); bar(); issueB(n + 7, 0); issueAh(t + 4, 2, 1);
        wait_vm<W>(); bar(); issueB(n + 8, 1); issueAh(t + 5, 0, 0);
      }
      wait_vm<W>(); bar(); issueB(29, 2); issueAh(15, 0, 1);
      wait_vm<W>(); bar(); issueB(30, 0);
      wait_vm<PB + PH>(); bar(); issueB(31, 1);
      wait_vm<PB>(); bar();
      wait_vm<0>(); bar();
    };
    if (lw < 2) body(std::integral_constant<int, 3>{}); else body(std::integral_constant<int, 2>{});
    return;
  }
  STAMP_DECL;
  STAMP0(FIRST ? 1 : 3, 0);
  const int fr = lane & 15, fq = lane >> 4;
  const int wm = w & 1, wn = w >> 1;
  const int offC = wm * 8192 + (fr >> 3) * 1024 + (fr & 7) * 128 + ((fq ^ (fr & 7)) << 4);
  const int offB = G2_B + wn * 5120 + lane * 16;
  f32x4 acc[4][5] = {};
  u32x4 xc[4], xb[5], yc[4], yb[5];
  auto rd = [&](u32x4 (&c)[4], u32x4 (&bb)[5], int sa, int sb, int h) {
    const char* pc = smem + sa * 16384 + (offC ^ (h << 6));
    const char* pb = smem + sb * 10240 + offB;
#pragma unroll
    for (int i = 0; i < 4; ++i) c[i] = *(const u32x4*)(pc + i * 2048);
#pragma unroll
    for (int n = 0; n < 5; ++n) bb[n] = *(const u32x4*)(pb + n * 1024);
  };
  auto mm = [&](u32x4 (&c)[4], u32x4 (&bb)[5]) {
#pragma unroll
    for (int n = 0; n < 5; ++n)
#pragma unroll
      for (int i = 0; i < 4; ++i) acc[i][n] = mfma(bb[n], c[i], acc[i][n]);
  };
  auto sched = [&]() {
#pragma unroll
    for (int q = 0; q < 9; ++q) { __builtin_amdgcn_sched_group_barrier(0x8, 1, 0); __builtin_amdgcn_sched_group_barrier(0x100, 1, 0); }
    __builtin_amdgcn_sched_group_barrier(0x8, 11, 0);
  };
  int sa = 0, sb = 0;
#pragma unroll
  for (int i = 0; i < 4; ++i)
#pragma unroll
    for (int n = 0; n < 5; ++n) asm volatile("" : "+v"(acc[i][n]));
  bar();
  STAMP0(FIRST ? 1 : 3, 1);
  rd(xc, xb, 0, 0, 0);
  for (int t = 0; t < 15; ++t) {
    sb = sb == 2 ? 0 : sb + 1;
    wait_lgkm0(); bar();
    __builtin_amdgcn_sched_barrier(0);
    rd(yc, yb, sa, sb, 1); mm(xc, xb); sched();
    __builtin_amdgcn_sched_barrier(0);
    sb = sb == 2 ? 0 : sb + 1;
    sa = sa == 2 ? 0 : sa + 1;
    wait_lgkm0(); bar();
    __builtin_amdgcn_sched_barrier(0);
    rd(xc, xb, sa, sb, 0); mm(yc, yb); sched();
    __builtin_amdgcn_sched_barrier(0);
  }
  wait_lgkm0(); bar();
  __builtin_amdgcn_sched_barrier(0);
  rd(yc, yb, 0, 1, 1); mm(xc, xb); sched();
  __builtin_amdgcn_sched_barrier(0);
  mm(yc, yb);
  STAMP0(FIRST ? 1 : 3, 2);
  const int t16 = mt * 8 + wm * 4, e16 = (nt * 2 + wn) * 5;
  const float sgn = half ? -1.f : 1.f;
  if (FIRST) {
    yield_flag((const unsigned*)(smem + G2_LDS - 16), 4);
    f32x4 xr[5];
#pragma unroll
    for (int n = 0; n < 5; ++n) {
      const u32x2 v = *(const u32x2*)(smem + G2_X0 + (wm * 16 + fr) * 320 + wn * 160 + n * 32 + fq * 8);
      xr[n] = sgn * f32x4{lo(v[0]), hi(v[0]), lo(v[1]), hi(v[1])};
    }
#pragma unroll
    for (int i = 0; i < 4; ++i)
      if (i == half * 2 + kq) {
#pragma unroll
        for (int n = 0; n < 5; ++n) acc[i][n] += xr[n];
      }
  }
  rsrc_t rP = mk(p.ws + (FIRST ? WS_P1 : WS_P3) + (half * 2 + kq) * SZ_X, SZ_X);
#pragma unroll
  for (int i = 0; i < 4; ++i)
#pragma unroll
    for (int n = 0; n < 5; ++n) {
      const f32x4 a = sgn * acc[i][n];
      __builtin_amdgcn_raw_buffer_store_b64(u32x2{pk2(a[0], a[1]), pk2(a[2], a[3])}, rP, (fq >> 1) * 256 + fr * 16 + (fq & 1) * 8,
                                            ((t16 + i) * 20 + ((e16 + n) >> 1)) * 1024 + ((e16 + n) & 1) * 512, P_AUX);
    }
  STAMP0(FIRST ? 1 : 3, 3);
  STAMP_FLUSH(FIRST ? 1 : 3, 0);
#endif
}

constexpr int FE_LDS = 32768;
#define FE_DELAY 50

__global__ __launch_bounds__(512, 4) void k_final(Params p) {
#if defined(__HIP_DEVICE_COMPILE__)
  extern __shared__ __attribute__((aligned(16))) char smem[];
  const int b = blockIdx.x;
  if (b >= 256) {
    const int g = b - 256;
    __builtin_amdgcn_s_sleep(FE_DELAY);
    if (g < NG_UP) ffn4_up(p, smem, g);
    else ffn4_down(p, smem, g - NG_UP);
    return;
  }
  const int tid = threadIdx.x, lane = tid & 63, fr = lane & 15, fq = lane >> 4;
  const int w = __builtin_amdgcn_readfirstlane(tid >> 6);
  rsrc_t rHO = mk(p.ws + WS_HO, HO_BYTES);
  const int t8 = (b & 4) * 32 + (b >> 3) * 4 + (b & 3);
  const int t = t8 * 8 + (lane & 7), hc = (lane >> 3) & 1;
  float* red = (float*)(smem + FL_RED);
  u32x4 x3[2];
  STAMP_DECL;
  STAMP0(5, 0);
  unsigned* cnt = (unsigned*)(smem + FL_TOP);
  if (tid < 2) cnt[tid] = 0;
  if (w < 5) {
    rsrc_t rP = mk(p.ws + WS_P3 + (t8 >> 1) * 20480, 4 * SZ_X - (t8 >> 1) * 20480), rX = mk(p.ws + WS_X1 + (t8 >> 1) * 20480, 20480), rW = mk(p.wr, 1280);
    u32x4 tf[8], pp[2][4], x1[2], wf[2];
    const int voff = hc * 1024 + fq * 256 + (t & 15) * 16;
#pragma unroll
    for (int i = 0; i < 2; ++i) {
#pragma unroll
      for (int a = 0; a < 4; ++a) pp[i][a] = ld16(rP, voff, a * SZ_X + (w * 4 + i * 2) * 1024);
      x1[i] = ld16(rX, voff, (w * 4 + i * 2) * 1024);
    }
    u32x4 cs = *(const u32x4*)(p.ws + WS_CS + t * 32 + (fq >> 1) * 16);
    const f32x2 st = *(const f32x2*)(p.ws + WS_STE + t * 8);
#pragma unroll
    for (int i = 0; i < 8; ++i) tf[i] = ld16(mk(p.ws + WS_T, 40960), lane * 16, w * 8192 + i * 1024);
#pragma unroll
    for (int i = 0; i < 2; ++i) wf[i] = ld16(rW, hc * 64 + fq * 16, (w * 4 + i * 2) * 64);
    const unsigned flip = (fq == 1 || fq == 2) ? 0x80008000u : 0u;
#pragma unroll
    for (int e = 0; e < 4; ++e) cs[e] ^= flip;
    const u32x4 sel0 = selector(lane, 0), sel1 = selector(lane, 1);
    f32x4 ya[2], yb[2];
#pragma unroll
    for (int i = 0; i < 2; ++i) {
      const f32x4 z = {};
      ya[i] = mfma(sel0, x1[i], z) * st[0] + st[1]; yb[i] = mfma(sel1, x1[i], z) * st[0] + st[1];
#pragma unroll
      for (int a = 0; a < 4; ++a) { ya[i] = mfma(sel0, pp[i][a], ya[i]); yb[i] = mfma(sel1, pp[i][a], yb[i]); }
    }
    f32x2 s = {0.f, 0.f}, q = {0.f, 0.f};
    float dw = 0.f;
#pragma unroll
    for (int i = 0; i < 2; ++i) {
      const f32x4 ca0 = mfma(tf[4 * i], cs, ya[i]), ca1 = mfma(tf[4 * i + 1], cs, yb[i]), cb0 = mfma(tf[4 * i + 2], cs, ya[i]), cb1 = mfma(tf[4 * i + 3], cs, yb[i]);
      const f32x4 y0 = hc ? cb0 : ca0, y1 = hc ? cb1 : ca1;
      x3[i] = u32x4{pk2(y0[0], y0[1]), pk2(y0[2], y0[3]), pk2(y1[0], y1[1]), pk2(y1[2], y1[3])};
#pragma unroll
      for (int e = 0; e < 4; ++e) { s[e & 1] = dot2(x3[i][e], 0x3f803f80u, s[e & 1]); q[e & 1] = dot2(x3[i][e], x3[i][e], q[e & 1]); dw = dot2(x3[i][e], wf[i][e], dw); }
    }
    const float s1 = xq_sum(s[0] + s[1]), q1 = xq_sum(q[0] + q[1]);
    dw = xq_sum(dw);
    if (fq == 0) *(f32x4*)(red + (w * 16 + fr) * 4) = f32x4{s1, q1, dw, 0.f};
  }
  const float sw = *(const float*)(p.ws + WS_HSUM + 5120);
  lds_bar();
  f32x4 tt = {};
#pragma unroll
  for (int q = 0; q < 10; ++q) tt += *(const f32x4*)(red + (q * 8 + (lane & 7)) * 4);
  const float mean = tt[0] * (1.f / 640.f), var = __builtin_fmaxf(tt[1] * (1.f / 640.f) - mean * mean, 0.f);
  const float mb = rb(mean), rs = __builtin_amdgcn_rcpf(rb(__builtin_amdgcn_sqrtf(rb(rb(var) + EPS))));
  const unsigned key = mkkey((tt[2] - mb * sw) * rs, t);
  if (tid < 8) __builtin_amdgcn_raw_buffer_store_b32(key, rHO, t * 4, HO_KEY3, 16);
  STAMP0(5, 1);
  u32x4 o[2];
  if (w < 5) {
#pragma unroll
    for (int i = 0; i < 2; ++i) {
#pragma unroll
      for (int e = 0; e < 4; ++e) {
        const unsigned v = x3[i][e];
        const unsigned xn = pk2(rb(lo(v) - mb) * rs, rb(hi(v) - mb) * rs);
        o[i][e] = pk2(lo(v) + lo(xn), hi(v) + hi(xn));
        x3[i][e] = nosent(v);
      }
      asm volatile("" : "+v"(o[i]), "+v"(x3[i]));
    }
  }
  u32x4 kk[1];
  poll16<1>(kk, rHO, tid * 16, HO_KEY3, 0);
  STAMP0(5, 2);
  {
    unsigned c[2] = {0u, 0u};
#pragma unroll
    for (int r = 0; r < 8; ++r) {
      const unsigned own = __builtin_amdgcn_readlane(key, r);
      const int n = popc(ballot(kk[0][0] > own)) + popc(ballot(kk[0][1] > own)) + popc(ballot(kk[0][2] > own)) + popc(ballot(kk[0][3] > own));
      c[r >> 2] += (unsigned)umin(n, 15) << ((r & 3) * 8);
    }
    if (lane < 2) __hip_atomic_fetch_add(cnt + lane, lane ? c[1] : c[0], __ATOMIC_RELAXED, __HIP_MEMORY_SCOPE_WORKGROUP);
  }
  lds_bar();
  STAMP0(5, 3);
  if (w < 5) {
    const int slot = (cnt[(lane >> 2) & 1] >> ((lane & 3) * 8)) & 255;
    if (slot < 8) {
#pragma unroll
      for (int i = 0; i < 2; ++i) __builtin_amdgcn_raw_buffer_store_b128(x3[i], rHO, hc * 512 + fq * 128 + slot * 16, HO_XR3 + (w * 4 + i * 2) * 512, 16);
      if (tid < 8) __builtin_amdgcn_raw_buffer_store_b128(u32x4{key, nosent(__builtin_bit_cast(unsigned, mb)), nosent(__builtin_bit_cast(unsigned, rs)), 0u}, rHO, slot * 16, HO_KS3, 16);
    }
    asm volatile("" ::: "memory");
    if (slot >= 8) {
#pragma unroll
      for (int i = 0; i < 2; ++i) *(u32x4*)((char*)p.out + t * 1280 + (w * 4 + i * 2 + hc) * 64 + fq * 16) = o[i];
    }
  }
  STAMP0(5, 4);
  STAMP_FLUSH(5, 0);
#endif
}

#if defined(__HIP_DEVICE_COMPILE__)
#pragma clang attribute pop
#endif

extern "C" void kernel_launch(void* const* d_in, const int* in_sizes, int n_in, void* d_out, int out_size, void* d_ws, size_t ws_size,
                              hipStream_t stream) {
  Params p;
  p.x0 = (const u16*)d_in[0]; p.Cr = (const u16*)d_in[1]; p.Ci = (const u16*)d_in[2]; p.Hr = (const u16*)d_in[3]; p.Hi = (const u16*)d_in[4];
  p.wr = (const u16*)d_in[6]; p.wg = (const u16*)d_in[7]; p.wu = (const u16*)d_in[8]; p.wd = (const u16*)d_in[9];
  p.out = (u16*)d_out; p.ws = (char*)d_ws;
  (void)hipFuncSetAttribute((const void*)k_gemm1<true>, hipFuncAttributeMaxDynamicSharedMemorySize, G1_LDS);
  (void)hipFuncSetAttribute((const void*)k_gemm1<false>, hipFuncAttributeMaxDynamicSharedMemorySize, G1_LDS);
  (void)hipFuncSetAttribute((const void*)k_gemm2<true>, hipFuncAttributeMaxDynamicSharedMemorySize, G2_LDS);
  (void)hipFuncSetAttribute((const void*)k_gemm2<false>, hipFuncAttributeMaxDynamicSharedMemorySize, G2_LDS);
  k_gemm1<true><<<256, 640, G1_LDS, stream>>>(p);
  k_gemm2<true><<<256, 768, G2_LDS, stream>>>(p);
  k_gemm1<false><<<256, 640, G1_LDS, stream>>>(p);
  k_gemm2<false><<<256, 768, G2_LDS, stream>>>(p);
  k_final<<<256 + NG_UP + NG_DOWN, 512, FE_LDS, stream>>>(p);
}
```
